```python
import torch

def _softmax(x, axis=-1):
    return torch.softmax(x, dim=axis)
def _gelu(x):
    return torch.nn.functional.gelu(x, approximate="tanh")
def _silu(x):
    return torch.nn.functional.silu(x)
def _sigmoid(x):
    return torch.sigmoid(x)
def _layernorm(x, axis=-1, eps=1e-5):
    m = x.mean(dim=axis, keepdim=True)
    v = x.var(dim=axis, keepdim=True, unbiased=False)
    return (x - m) / torch.sqrt(v + eps)
def _scatter_add(tgt, idx, src, axis=0):
    return tgt.clone().index_add_(axis, idx, src)
def _segment_sum(src, seg, n):
    out = torch.zeros((n,) + src.shape[1:], dtype=src.dtype)
    return out.index_add_(0, seg, src)
def _pad(x, pw):
    flat = tuple(v for pair in reversed(pw) for v in pair)
    return torch.nn.functional.pad(x, flat)

kernel_name = 'gated_mix__gqa_block_cached__dense_activation_squared_relu__decode'

def setup_inputs():

    _w = (lambda a: (lambda t: t.to(torch.bfloat16) if t.is_floating_point() else t)(torch.as_tensor(a)))
    def _A_setup():
        B, Sq, D = 8, 1, 512
        Hq, Hkv, Sk, Dh = 32, 8, 4096, 128
        assert Hq % Hkv == 0, (Hq, Hkv)
        return {
            "x": _w((lambda *s: torch.randn(*s, dtype=torch.bfloat16) * 0.05)(B, Sq, D)),
            "k_cache": _w((lambda *s: torch.randn(*s, dtype=torch.bfloat16) * 6.4)(B, Hkv, Sk, Dh)),
            "v_cache": _w((lambda *s: torch.randn(*s, dtype=torch.bfloat16) * 0.4)(B, Hkv, Sk, Dh)),
            "wq": _w((lambda *s: torch.randn(*s, dtype=torch.bfloat16) * 6.4)(D, Hq * Dh) * (D ** -0.5)),
            "wo": _w((lambda *s: torch.randn(*s, dtype=torch.bfloat16) * 0.4)(Hq * Dh, D) * ((Hq * Dh) ** -0.5)),
        }
    _A_in = _A_setup()

    def _B_setup():
        B, S, D, F = 8, 1, 512, 1792
        return {
            "x": _w((lambda *s: torch.randn(*s, dtype=torch.bfloat16) * 0.05)(B, S, D)),
            "w1": _w((lambda *s: torch.randn(*s, dtype=torch.bfloat16) * 0.05)(D, F)),
            "b1": _w((lambda *s: torch.randn(*s, dtype=torch.bfloat16) * 0.05)(F)),
            "w2": _w((lambda *s: torch.randn(*s, dtype=torch.bfloat16) * 0.05)(F, D)),
            "b2": _w((lambda *s: torch.randn(*s, dtype=torch.bfloat16) * 0.05)(D)),
        }
    _B_in = _B_setup()

    return {
        "A_x": _A_in["x"],
        "A_k_cache": _A_in["k_cache"],
        "A_v_cache": _A_in["v_cache"],
        "A_wq": _A_in["wq"],
        "A_wo": _A_in["wo"],
        "B_x": _B_in["x"],
        "B_w1": _B_in["w1"],
        "B_b1": _B_in["b1"],
        "B_w2": _B_in["w2"],
        "B_b2": _B_in["b2"],
        "gate_w": _w((lambda *s: torch.randn(*s, dtype=torch.bfloat16) * 0.05)(512) * 16.0),
    }

def reference(A_x, A_k_cache, A_v_cache, A_wq, A_wo, B_x, B_w1, B_b1, B_w2, B_b2, gate_w):
    def _A_ref(x, k_cache, v_cache, wq, wo):
        B, Sq, D = x.shape
        Hq, Dh = 32, 128
        Hkv = k_cache.shape[1]
        rep = Hq // Hkv
        q = (lambda x, *d: x.permute(*d))((x @ wq).reshape(B, Sq, Hq, Dh), 0, 2, 1, 3)
        k = (lambda x, n, axis: torch.repeat_interleave(x, n, dim=axis))(k_cache, rep, axis=1)
        v = (lambda x, n, axis: torch.repeat_interleave(x, n, dim=axis))(v_cache, rep, axis=1)
        scores = torch.einsum("bhqd,bhkd->bhqk", q, k) * (Dh ** -0.5)
        p = _softmax(scores, axis=-1)
        o = torch.einsum("bhqk,bhkd->bhqd", p, v)
        return (lambda x, *d: x.permute(*d))(o, 0, 2, 1, 3).reshape(B, Sq, Hq * Dh) @ wo

    def _B_ref(x, w1, b1, w2, b2):
        h = torch.einsum("bsd,df->bsf", x, w1) + b1
        zero = torch.zeros_like(h)
        r = torch.where(h > zero, h, zero)
        h = r * r
        out = torch.einsum("bsf,fd->bsd", h, w2) + b2
        return out

    x = A_x
    g = _sigmoid((x * gate_w).sum(-1, keepdims=True))
    a = _A_ref(x, A_k_cache, A_v_cache, A_wq, A_wo)
    b = _B_ref(x, B_w1, B_b1, B_w2, B_b2)
    return g * a + (1 - g) * b
```

```cpp
#include <hip/hip_runtime.h>
#include <hip/hip_bf16.h>
#include <stdint.h>

#define KWIN 6
#define QPOS 24
#define VWIN 0
#define KVAUX 2
#define WAUX 0

typedef unsigned v4u __attribute__((ext_vector_type(4)));
typedef unsigned v2u __attribute__((ext_vector_type(2)));
typedef float f32x4 __attribute__((ext_vector_type(4)));
typedef float f32x2 __attribute__((ext_vector_type(2)));
typedef short s16x4 __attribute__((ext_vector_type(4)));
typedef __bf16 bf16x8 __attribute__((ext_vector_type(8)));
typedef __bf16 bf16x2 __attribute__((ext_vector_type(2)));
#define LAS __attribute__((address_space(3)))
#define DI __device__ __forceinline__

constexpr int SC1 = 16;
constexpr int RSRC3 = 0x00020000;
constexpr unsigned WS_QG = 4096;
constexpr unsigned WS_HG = WS_QG + 8 * 2048 * 8;
constexpr unsigned WS_OG = WS_HG + 65536;
constexpr unsigned WS_LG = WS_OG + 32 * 32 * 128 * 8;
constexpr unsigned WS_PG = WS_LG + 32 * 32 * 8;
constexpr unsigned WS_FG = WS_PG + 256 * 32 * 16 * 8;
constexpr unsigned WS_END = WS_FG + 256 * 16 * 16 * 8;
constexpr int XS_STR = 1040, TP_STR = 160, OS_STR = 272;
constexpr int L_XS = 0, L_TQ = 8 * XS_STR, L_T2 = L_TQ + 16384, L_TO = L_T2 + 128 * TP_STR, L_OS = L_TO + 128 * TP_STR,
              L_ML = L_OS + 8 * OS_STR, L_END = L_ML + 256;
constexpr int L_MRG = L_TQ;
constexpr unsigned SPIN_CAP = 1u << 20;

DI f32x4 mfma32(v4u a, v4u b, f32x4 c) {
  return __builtin_amdgcn_mfma_f32_16x16x32_bf16(__builtin_bit_cast(bf16x8, a), __builtin_bit_cast(bf16x8, b), c, 0, 0, 0);
}
DI f32x4 mfma16(v2u a, v2u b, f32x4 c) {
  return __builtin_amdgcn_mfma_f32_16x16x16bf16_1k(__builtin_bit_cast(s16x4, a), __builtin_bit_cast(s16x4, b), c, 0, 0, 0);
}
template <int CTRL> DI float dpp(float x) {
  return __builtin_bit_cast(float, __builtin_amdgcn_mov_dpp(__builtin_bit_cast(int, x), CTRL, 0xf, 0xf, true));
}
DI unsigned pk_bf16(float lo, float hi) {
  f32x2 v = {lo, hi};
  return __builtin_bit_cast(unsigned, __builtin_convertvector(v, bf16x2));
}
DI float rbf(float x) { return __builtin_bit_cast(float, pk_bf16(x, x) & 0xffff0000u); }
DI float bf_lo(unsigned u) { return __builtin_bit_cast(float, u << 16); }
DI float bf_hi(unsigned u) { return __builtin_bit_cast(float, u & 0xffff0000u); }
DI float u2f(unsigned u) { return __builtin_bit_cast(float, u); }
DI unsigned f2u(float f) { return __builtin_bit_cast(unsigned, f); }
DI float xrow_max(float x) {
  auto s = __builtin_amdgcn_permlane16_swap(f2u(x), f2u(x), false, false);
  x = fmaxf(u2f(s[0]), u2f(s[1]));
  auto t = __builtin_amdgcn_permlane32_swap(f2u(x), f2u(x), false, false);
  return fmaxf(u2f(t[0]), u2f(t[1]));
}
DI float xrow_sum(float x) {
  auto s = __builtin_amdgcn_permlane16_swap(f2u(x), f2u(x), false, false);
  x = u2f(s[0]) + u2f(s[1]);
  auto t = __builtin_amdgcn_permlane32_swap(f2u(x), f2u(x), false, false);
  return u2f(t[0]) + u2f(t[1]);
}
DI __amdgpu_buffer_rsrc_t rsrc(const void* p, unsigned bytes) {
  return __builtin_amdgcn_make_buffer_rsrc(const_cast<void*>(p), 0, bytes, RSRC3);
}
template <int AUX> DI v4u ld128(__amdgpu_buffer_rsrc_t r, unsigned voff, unsigned soff) {
  return __builtin_amdgcn_raw_buffer_load_b128(r, voff, soff, AUX);
}
template <int AUX> DI v2u ld64(__amdgpu_buffer_rsrc_t r, unsigned voff, unsigned soff) {
  return __builtin_amdgcn_raw_buffer_load_b64(r, voff, soff, AUX);
}
DI void st128(__amdgpu_buffer_rsrc_t r, unsigned voff, unsigned soff, v4u v) { __builtin_amdgcn_raw_buffer_store_b128(v, r, voff, soff, SC1); }
DI void st64(__amdgpu_buffer_rsrc_t r, unsigned voff, unsigned soff, v2u v) { __builtin_amdgcn_raw_buffer_store_b64(v, r, voff, soff, SC1); }
DI v2u trread(LAS unsigned char* p) {
  return __builtin_bit_cast(v2u, __builtin_amdgcn_ds_read_tr16_b64_v4i16((LAS s16x4*)p));
}
DI bool tags_ok(v4u g, unsigned T) { return (g[1] == T) & (g[3] == T); }
DI void wg_barrier() {
  asm volatile("s_waitcnt lgkmcnt(0)" ::: "memory");
  __builtin_amdgcn_s_barrier();
  asm volatile("" ::: "memory");
}
template <int N> DI void wait_vm() { asm volatile("s_waitcnt vmcnt(%0)" ::"i"(N) : "memory"); }
#define SCHED_FENCE() __builtin_amdgcn_sched_barrier(0)

DI f32x4 tile_512x16(LAS unsigned char* tile, LAS unsigned char* xs, int lane) {
  const int g = lane >> 4, q4 = (lane & 15) >> 2, p = lane & 3;
  LAS unsigned char* ta = tile + (8 * g + q4) * 32 + 8 * p;
  LAS unsigned char* xb = xs + (lane & 7) * XS_STR + 16 * g;
  f32x4 c0 = {0, 0, 0, 0}, c1 = {0, 0, 0, 0};
#pragma unroll
  for (int s = 0; s < 16; s += 2) {
    v2u a0 = trread(ta + s * 1024), a1 = trread(ta + s * 1024 + 128);
    v2u a2 = trread(ta + s * 1024 + 1024), a3 = trread(ta + s * 1024 + 1024 + 128);
    v4u b0 = *(LAS v4u*)(xb + s * 64), b1 = *(LAS v4u*)(xb + s * 64 + 64);
    c0 = mfma32(v4u{a0[0], a0[1], a1[0], a1[1]}, b0, c0);
    c1 = mfma32(v4u{a2[0], a2[1], a3[0], a3[1]}, b1, c1);
  }
  return c0 + c1;
}
DI v4u frag_128x64(LAS unsigned char* tile, int lane, int ks, int cq) {
  const int g = lane >> 4, q4 = (lane & 15) >> 2, p = lane & 3;
  LAS unsigned char* ta = tile + (32 * ks + 8 * g + q4) * TP_STR + 32 * cq + 8 * p;
  v2u a0 = trread(ta), a1 = trread(ta + 4 * TP_STR);
  return v4u{a0[0], a0[1], a1[0], a1[1]};
}

__global__ void __launch_bounds__(512, 2)
fused_decode(const uint16_t* x, const uint16_t* kc, const uint16_t* vc, const uint16_t* wq, const uint16_t* wo, const uint16_t* w1,
             const uint16_t* b1, const uint16_t* w2, const uint16_t* b2, const uint16_t* gw, uint16_t* out, unsigned char* ws) {
  __shared__ __attribute__((aligned(16))) unsigned char lds_raw[L_END];
  LAS unsigned char* lds = (LAS unsigned char*)lds_raw;
  const int j = blockIdx.x, tid = threadIdx.x, lane = tid & 63;
  const int w = __builtin_amdgcn_readfirstlane(tid >> 6);
  const int x8 = j & 7, r32 = j >> 3;
  const int kvh = x8, b = r32 >> 2, sp = r32 & 3;
  const bool isH = r32 < 14, isF = (r32 >= 14) & (r32 < 28);

  const unsigned T = *(const unsigned*)ws * 1664525u + 1013904223u;
  const __amdgpu_buffer_rsrc_t rws = rsrc(ws, WS_END);

  const v4u lx = ld128<0>(rsrc(x, 8192), lane * 16, w * 1024);
  const int n0 = 512 * x8 + 16 * r32;
  v4u lq[2], l2[2], lo[2];
  {
    const __amdgpu_buffer_rsrc_t r = rsrc(wq, 512 * 4096 * 2);
#pragma unroll
    for (int i = 0; i < 2; ++i) lq[i] = ld128<WAUX>(r, (lane >> 1) * 8192 + (lane & 1) * 16, (64 * w + 32 * i) * 8192 + n0 * 2);
  }
  const int ht = 14 * x8 + r32;
  const int fks = r32 - 14;
  if (isH) {
    const __amdgpu_buffer_rsrc_t r = rsrc(w1, 512 * 1792 * 2);
#pragma unroll
    for (int i = 0; i < 2; ++i) l2[i] = ld128<WAUX>(r, (lane >> 1) * 3584 + (lane & 1) * 16, (64 * w + 32 * i) * 3584 + ht * 32);
  } else if (isF) {
    const __amdgpu_buffer_rsrc_t r = rsrc(w2, 1792 * 512 * 2);
#pragma unroll
    for (int i = 0; i < 2; ++i) l2[i] = ld128<WAUX>(r, (lane >> 3) * 1024 + (lane & 7) * 16, (128 * fks + 16 * w + 8 * i) * 1024 + x8 * 128);
  } else {
    l2[0] = l2[1] = v4u{0, 0, 0, 0};
  }
  {
    const __amdgpu_buffer_rsrc_t r = rsrc(wo, 4096 * 512 * 2);
#pragma unroll
    for (int i = 0; i < 2; ++i) lo[i] = ld128<WAUX>(r, (lane >> 3) * 1024 + (lane & 7) * 16, (128 * r32 + 16 * w + 8 * i) * 1024 + x8 * 128);
  }
  v4u lsm = {0, 0, 0, 0};
  unsigned lb2 = 0;
  if (w == 1 && isH) {
    v2u t = ld64<0>(rsrc(b1, 1792 * 2), (lane >> 4) * 8, ht * 32);
    lsm[0] = t[0]; lsm[1] = t[1];
  } else if (w == 4) {
    lsm = ld128<0>(rsrc(gw, 1024), lane * 16, 0);
    lb2 = __builtin_amdgcn_raw_buffer_load_b32(rsrc(b2, 1024), (lane & 7) * 4, (64 * x8 + 16 * sp) * 2, 0);
  }
  SCHED_FENCE();

  const size_t kvoff = ((size_t)(b * 8 + kvh) * 4096 + sp * 1024 + w * 128) * 128;
  const __amdgpu_buffer_rsrc_t rk = rsrc(kc + kvoff, 32768), rv = rsrc(vc + kvoff, 32768);
  const unsigned koff = ((lane & 15) >> 2) * 256 + (lane >> 4) * 64 + (lane & 3) * 16;
  v4u kv[32];
#pragma unroll
  for (int i = 0; i < KWIN; ++i) kv[i] = ld128<KVAUX>(rk, koff, i * 1024);
  SCHED_FENCE();

  *(LAS v4u*)(lds + L_XS + w * XS_STR + lane * 16) = lx;
#pragma unroll
  for (int i = 0; i < 2; ++i) *(LAS v4u*)(lds + L_TQ + (64 * w + 32 * i) * 32 + lane * 16) = lq[i];
  if (isH) {
#pragma unroll
    for (int i = 0; i < 2; ++i) *(LAS v4u*)(lds + L_T2 + (64 * w + 32 * i) * 32 + lane * 16) = l2[i];
  } else {
#pragma unroll
    for (int i = 0; i < 2; ++i) *(LAS v4u*)(lds + L_T2 + (16 * w + 8 * i + (lane >> 3)) * TP_STR + (lane & 7) * 16) = l2[i];
  }
#pragma unroll
  for (int i = 0; i < 2; ++i) *(LAS v4u*)(lds + L_TO + (16 * w + 8 * i + (lane >> 3)) * TP_STR + (lane & 7) * 16) = lo[i];
  wg_barrier();

  float gate = 0.f;
  if (w == 0) {
    f32x4 c = tile_512x16(lds + L_TQ, lds + L_XS, lane);
    if ((lane & 15) < 8)
      st128(rws, (lane & 15) * 16384 + (lane >> 4) * 16, WS_QG + n0 * 4, v4u{pk_bf16(c[0], c[1]), T, pk_bf16(c[2], c[3]), T});
  } else if (w == 1) {
    if (isH) {
      f32x4 c = tile_512x16(lds + L_T2, lds + L_XS, lane);
      float h[4];
      h[0] = rbf(rbf(c[0]) + bf_lo(lsm[0])); h[1] = rbf(rbf(c[1]) + bf_hi(lsm[0]));
      h[2] = rbf(rbf(c[2]) + bf_lo(lsm[1])); h[3] = rbf(rbf(c[3]) + bf_hi(lsm[1]));
#pragma unroll
      for (int r = 0; r < 4; ++r) { h[r] = h[r] > 0.f ? h[r] : 0.f; h[r] = h[r] * h[r]; }
      if ((lane & 15) < 8)
        st128(rws, (lane & 15) * (896 * 8) + (lane >> 4) * 16, WS_HG + ht * 64, v4u{pk_bf16(h[0], h[1]), T, pk_bf16(h[2], h[3]), T});
    }
  } else if (w == 4) {
    const v4u xv = *(LAS v4u*)(lds + L_XS + b * XS_STR + lane * 16);
    float t = 0.f;
#pragma unroll
    for (int e = 0; e < 4; ++e) t += rbf(bf_lo(xv[e]) * bf_lo(lsm[e])) + rbf(bf_hi(xv[e]) * bf_hi(lsm[e]));
    t += dpp<0xB1>(t); t += dpp<0x4E>(t); t += dpp<0x141>(t); t += dpp<0x140>(t);
    t = rbf(xrow_sum(t));
    gate = rbf(1.f / (1.f + __expf(-t)));
  }
  SCHED_FENCE();

  v4u qg[2], hg[8];
  const unsigned qoff = (((lane & 15) >> 2) * 64 + (lane >> 4) * 16 + (lane & 3) * 4) * 8;
  const unsigned qsoff = WS_QG + (b * 2048 + kvh * 256) * 8;
  const unsigned hoff = (lane & 7) * (896 * 8) + (lane >> 4) * 32;
  const unsigned hsoff = WS_HG + fks * 512;
  const bool doF = isF && w == 1;
#pragma unroll
  for (int i = KWIN; i < 32; ++i) {
    if (i == QPOS) {
      qg[0] = ld128<SC1>(rws, qoff, qsoff);
      qg[1] = ld128<SC1>(rws, qoff + 16, qsoff);
      if (doF) {
#pragma unroll
        for (int s = 0; s < 4; ++s) {
          hg[2 * s] = ld128<SC1>(rws, hoff + s * 128, hsoff);
          hg[2 * s + 1] = ld128<SC1>(rws, hoff + s * 128 + 16, hsoff);
        }
      } else {
#pragma unroll
        for (int s = 0; s < 8; ++s) hg[s] = v4u{0, T, 0, T};
      }
      SCHED_FENCE();
    }
    wait_vm<KWIN - 1>();
    kv[i] = ld128<KVAUX>(rk, koff, i * 1024);
    SCHED_FENCE();
  }

  {
    bool ok = tags_ok(qg[0], T) & tags_ok(qg[1], T);
#pragma unroll
    for (int s = 0; s < 8; ++s) ok &= tags_ok(hg[s], T);
    if (!__all(ok)) {
      for (unsigned spin = 0; spin < SPIN_CAP; ++spin) {
        __builtin_amdgcn_s_sleep(2);
        qg[0] = ld128<SC1>(rws, qoff, qsoff);
        qg[1] = ld128<SC1>(rws, qoff + 16, qsoff);
        if (doF) {
#pragma unroll
          for (int s = 0; s < 4; ++s) {
            hg[2 * s] = ld128<SC1>(rws, hoff + s * 128, hsoff);
            hg[2 * s + 1] = ld128<SC1>(rws, hoff + s * 128 + 16, hsoff);
          }
        }
        ok = tags_ok(qg[0], T) & tags_ok(qg[1], T);
#pragma unroll
        for (int s = 0; s < 8; ++s) ok &= tags_ok(hg[s], T);
        asm volatile("" ::: "memory");
        if (__all(ok)) break;
      }
    }
  }
  const v4u qf = {qg[0][0], qg[0][2], qg[1][0], qg[1][2]};
  v4u hf[4];
#pragma unroll
  for (int s = 0; s < 4; ++s) hf[s] = v4u{hg[2 * s][0], hg[2 * s][2], hg[2 * s + 1][0], hg[2 * s + 1][2]};
  SCHED_FENCE();

  float S[32];
#pragma unroll
  for (int i = 0; i < 32; ++i) {
    const f32x4 d = mfma32(kv[i], qf, f32x4{0, 0, 0, 0});
    S[i] = (dpp<0x00>(d[0]) + dpp<0x55>(d[1])) + (dpp<0xAA>(d[2]) + dpp<0xFF>(d[3]));
    SCHED_FENCE();
    if (VWIN > 0) wait_vm<(VWIN > 0 ? VWIN - 1 : 0)>();
    kv[i] = ld128<KVAUX>(rv, lane * 16, i * 1024);
    SCHED_FENCE();
  }
  constexpr float C2 = 0.08838834764831845f * 1.4426950408889634f;
  float m = S[0];
#pragma unroll
  for (int i = 1; i < 32; ++i) m = fmaxf(m, S[i]);
  m = xrow_max(m) * C2;
  float l = 0.f;
  unsigned pp[32];
#pragma unroll
  for (int i = 0; i < 32; ++i) {
    const float p = __builtin_amdgcn_exp2f(__builtin_fmaf(S[i], C2, -m));
    l += p;
    pp[i] = pk_bf16(p, p);
  }
  l = xrow_sum(l);
  if ((lane & 0x33) == 0) {
    *(LAS float*)(lds + L_ML + (lane >> 2) * 32 + w * 4) = m;
    *(LAS float*)(lds + L_ML + 128 + (lane >> 2) * 32 + w * 4) = l;
  }
  SCHED_FENCE();

  if (doF) {
#pragma unroll
    for (int cq = 0; cq < 4; ++cq) {
      f32x4 c = {0, 0, 0, 0};
#pragma unroll
      for (int s = 0; s < 4; ++s) c = mfma32(frag_128x64(lds + L_T2, lane, s, cq), hf[s], c);
      if ((lane & 15) < 8) {
        const unsigned vo = ((lane & 15) * 4 * 256 + (lane >> 4) * 4) * 8, so = WS_FG + ((x8 * 32 + cq) * 256 + fks * 16) * 8;
        st128(rws, vo, so, v4u{f2u(c[0]), T, f2u(c[1]), T});
        st128(rws, vo + 16, so, v4u{f2u(c[2]), T, f2u(c[3]), T});
      }
    }
  }
  SCHED_FENCE();

  const int cc = lane & 3;
  const unsigned mask0 = cc == 0 ? 0x0000ffffu : cc == 1 ? 0xffff0000u : 0u;
  const unsigned mask1 = cc == 2 ? 0x0000ffffu : cc == 3 ? 0xffff0000u : 0u;
  f32x4 a0 = {0, 0, 0, 0}, a1 = {0, 0, 0, 0};
#pragma unroll
  for (int i = 0; i < 32; ++i) {
    const v2u pa = {pp[i] & mask0, pp[i] & mask1};
    a0 = mfma16(pa, v2u{kv[i][0], kv[i][1]}, a0);
    a1 = mfma16(pa, v2u{kv[i][2], kv[i][3]}, a1);
  }
  *(LAS f32x4*)(lds + L_MRG + w * 2048 + lane * 32) = a0;
  *(LAS f32x4*)(lds + L_MRG + w * 2048 + lane * 32 + 16) = a1;
  wg_barrier();

  v4u wf[4];
  if (w < 4) {
    const f32x4 m0 = *(LAS f32x4*)(lds + L_ML + w * 32), m1 = *(LAS f32x4*)(lds + L_ML + w * 32 + 16);
    const f32x4 l0 = *(LAS f32x4*)(lds + L_ML + 128 + w * 32), l1 = *(LAS f32x4*)(lds + L_ML + 128 + w * 32 + 16);
    f32x2 ov[8];
#pragma unroll
    for (int u = 0; u < 8; ++u) ov[u] = *(LAS f32x2*)(lds + L_MRG + u * 2048 + w * 512 + lane * 8);
    const float M = fmaxf(fmaxf(fmaxf(m0[0], m0[1]), fmaxf(m0[2], m0[3])), fmaxf(fmaxf(m1[0], m1[1]), fmaxf(m1[2], m1[3])));
    float L = 0.f, o0 = 0.f, o1 = 0.f;
#pragma unroll
    for (int u = 0; u < 8; ++u) {
      const float wt = __builtin_amdgcn_exp2f((u < 4 ? m0[u & 3] : m1[u & 3]) - M);
      L = __builtin_fmaf(wt, u < 4 ? l0[u & 3] : l1[u & 3], L);
      o0 = __builtin_fmaf(wt, ov[u][0], o0);
      o1 = __builtin_fmaf(wt, ov[u][1], o1);
    }
    const float inv = __builtin_amdgcn_rcpf(L);
    const unsigned slot = ((4 * kvh + w) * 8 + b) * 4 + sp;
    st128(rws, lane * 16, WS_OG + slot * 1024, v4u{f2u(o0 * inv), T, f2u(o1 * inv), T});
    if (lane == 0) st64(rws, 0, WS_LG + slot * 8, v2u{f2u(M + __builtin_amdgcn_logf(L)), T});
#pragma unroll
    for (int s = 0; s < 4; ++s) wf[s] = frag_128x64(lds + L_TO, lane, s, w);
  } else {
#pragma unroll
    for (int s = 0; s < 4; ++s) wf[s] = v4u{0, 0, 0, 0};
  }

  {
    const unsigned so = WS_OG + (r32 * 8 + w) * 4096, sl = WS_LG + (r32 * 8 + w) * 32;
    v4u og[4];
    v2u lg;
    for (unsigned spin = 0; spin < SPIN_CAP; ++spin) {
#pragma unroll
      for (int s = 0; s < 4; ++s) og[s] = ld128<SC1>(rws, lane * 16 + s * 1024, so);
      lg = ld64<SC1>(rws, cc * 8, sl);
      bool ok = lg[1] == T;
#pragma unroll
      for (int s = 0; s < 4; ++s) ok &= tags_ok(og[s], T);
      asm volatile("" ::: "memory");
      if (__all(ok)) break;
      __builtin_amdgcn_s_sleep(1);
    }
    const float ls = u2f(lg[0]);
    float M = fmaxf(ls, dpp<0xB1>(ls));
    M = fmaxf(M, dpp<0x4E>(M));
    const float e = __builtin_amdgcn_exp2f(ls - M);
    float Z = e + dpp<0xB1>(e);
    Z += dpp<0x4E>(Z);
    const float wn = e * __builtin_amdgcn_rcpf(Z);
    const float w0 = dpp<0x00>(wn), w1_ = dpp<0x55>(wn), w2_ = dpp<0xAA>(wn), w3 = dpp<0xFF>(wn);
    const float o0 = w0 * u2f(og[0][0]) + w1_ * u2f(og[1][0]) + w2_ * u2f(og[2][0]) + w3 * u2f(og[3][0]);
    const float o1 = w0 * u2f(og[0][2]) + w1_ * u2f(og[1][2]) + w2_ * u2f(og[2][2]) + w3 * u2f(og[3][2]);
    *(LAS unsigned*)(lds + L_OS + w * OS_STR + lane * 4) = pk_bf16(o0, o1);
  }
  wg_barrier();

  if (w < 4) {
    f32x4 c = {0, 0, 0, 0};
#pragma unroll
    for (int s = 0; s < 4; ++s) c = mfma32(wf[s], *(LAS v4u*)(lds + L_OS + (lane & 7) * OS_STR + s * 64 + (lane >> 4) * 16), c);
    if ((lane & 15) < 8) {
      const unsigned vo = ((lane & 15) * 4 * 512 + (lane >> 4) * 4) * 8, so = WS_PG + ((x8 * 32 + w) * 512 + r32 * 16) * 8;
      st128(rws, vo, so, v4u{f2u(c[0]), T, f2u(c[1]), T});
      st128(rws, vo + 16, so, v4u{f2u(c[2]), T, f2u(c[3]), T});
    }
  } else if (w == 4) {
    const unsigned sp_ = WS_PG + (x8 * 32 + r32) * 4096, sf = WS_FG + (x8 * 32 + r32) * 2048;
    const bool f1live = lane < 48;
    v4u pg[4], fg[2];
    for (unsigned spin = 0; spin < SPIN_CAP; ++spin) {
#pragma unroll
      for (int i = 0; i < 4; ++i) pg[i] = ld128<SC1>(rws, lane * 16 + i * 1024, sp_);
      fg[0] = ld128<SC1>(rws, lane * 16, sf);
      fg[1] = ld128<SC1>(rws, lane * 16 + 1024, sf);
      bool ok = tags_ok(fg[0], T) & (tags_ok(fg[1], T) | !f1live);
#pragma unroll
      for (int i = 0; i < 4; ++i) ok &= tags_ok(pg[i], T);
      asm volatile("" ::: "memory");
      if (__all(ok)) break;
      __builtin_amdgcn_s_sleep(1);
    }
    float s0 = (u2f(pg[0][0]) + u2f(pg[1][0])) + (u2f(pg[2][0]) + u2f(pg[3][0]));
    float s1 = (u2f(pg[0][2]) + u2f(pg[1][2])) + (u2f(pg[2][2]) + u2f(pg[3][2]));
    float f0 = u2f(fg[0][0]) + (f1live ? u2f(fg[1][0]) : 0.f);
    float f1 = u2f(fg[0][2]) + (f1live ? u2f(fg[1][2]) : 0.f);
    s0 += dpp<0x128>(s0); s1 += dpp<0x128>(s1); f0 += dpp<0x128>(f0); f1 += dpp<0x128>(f1);
    s0 = xrow_sum(s0); s1 = xrow_sum(s1); f0 = xrow_sum(f0); f1 = xrow_sum(f1);
    const float og = rbf(1.f - gate);
    const float r0 = rbf(gate * rbf(s0)) + rbf(og * rbf(rbf(f0) + bf_lo(lb2)));
    const float r1 = rbf(gate * rbf(s1)) + rbf(og * rbf(rbf(f1) + bf_hi(lb2)));
    if (lane < 8) *(unsigned*)(out + b * 512 + 64 * x8 + 16 * sp + 2 * lane) = pk_bf16(r0, r1);
    if (j == 0 && lane == 0) *(unsigned*)ws = T;
  }
}

extern "C" void kernel_launch(void* const* d_in, const int* in_sizes, int n_in, void* d_out, int out_size, void* d_ws, size_t ws_size,
                              hipStream_t stream) {
  fused_decode<<<256, 512, 0, stream>>>((const uint16_t*)d_in[0], (const uint16_t*)d_in[1], (const uint16_t*)d_in[2],
                                        (const uint16_t*)d_in[3], (const uint16_t*)d_in[4], (const uint16_t*)d_in[6],
                                        (const uint16_t*)d_in[7], (const uint16_t*)d_in[8], (const uint16_t*)d_in[9],
                                        (const uint16_t*)d_in[10], (uint16_t*)d_out, (unsigned char*)d_ws);
}
```

```cpp
#include <hip/hip_runtime.h>
#include <hip/hip_bf16.h>
#include <stdint.h>

constexpr int WIN = 5;
constexpr int NSH = 30, NSL = 64 - NSH;
constexpr int NRH = 30, NRL = 36;
constexpr int G1H = 4, G1L = 12;
constexpr int GPOS = 18;
constexpr int HP = 3;
constexpr int TEND = 1876;
constexpr int TH0 = 320, TL0 = 130;
constexpr int AL0 = 105;
constexpr int OB = 36;
constexpr int C2L = 108;
constexpr int CODEB = 28672;

typedef unsigned v4u __attribute__((ext_vector_type(4)));
typedef unsigned v2u __attribute__((ext_vector_type(2)));
typedef float f32x4 __attribute__((ext_vector_type(4)));
typedef float f32x2 __attribute__((ext_vector_type(2)));
typedef short s16x4 __attribute__((ext_vector_type(4)));
typedef __bf16 bf16x8 __attribute__((ext_vector_type(8)));
typedef __bf16 bf16x2 __attribute__((ext_vector_type(2)));
typedef unsigned long long u64x8 __attribute__((ext_vector_type(8)));
typedef unsigned long long u64x4 __attribute__((ext_vector_type(4)));
#define LAS __attribute__((address_space(3)))
#define DI __device__ __forceinline__
#define INL __attribute__((always_inline))
#define SCHED_FENCE() __builtin_amdgcn_sched_barrier(0)
#define SPIN(cap) _Pragma("nounroll") for (unsigned spin = 0; spin < (cap); ++spin)

constexpr int NT = 2, SC1 = 16;
constexpr unsigned OOB = 0x7fffff00u;
constexpr unsigned CAP_G = 1u << 21, CAP_L = 1u << 25;
constexpr unsigned WS_T0 = 4096;
constexpr unsigned WS_QG = 8192;
constexpr unsigned WS_HG = WS_QG + 8 * 2048 * 8;
constexpr unsigned WS_OG = WS_HG + 65536;
constexpr unsigned WS_LG = WS_OG + 32 * 32 * 64 * 8;
constexpr unsigned WS_PG = WS_LG + 256 * 128;
constexpr unsigned WS_FG = WS_PG + 256 * 32 * 16 * 8;
constexpr unsigned WS_H2 = WS_FG + 256 * 4 * 16 * 8;
constexpr unsigned WS_END = WS_H2 + 256 * 128;
constexpr int TP_STR = 160, OS_STR = 272, STG = 64 * TP_STR;
constexpr int L_STG = 0;
constexpr int L_TO = 8 * STG;
constexpr int L_MRG = L_TO + 128 * TP_STR;
constexpr int L_OS = L_MRG + 8 * 2048;
constexpr int L_QS = L_OS + 8 * OS_STR;
constexpr int L_ML = L_QS + 1024;
constexpr int L_CNT = L_ML + 400;
constexpr int L_END = L_CNT + 64;
enum { C_TILE = 0, C_ANN1 = 4, C_ANN2 = 8, C_QRDY = 12, C_FFN = 16, C_QLEAD = 20, C_SOFT = 24, C_REP = 28, C_RLEAD = 32, C_ORG = 36 };

DI f32x4 mfma32(v4u a, v4u b, f32x4 c) {
  return __builtin_amdgcn_mfma_f32_16x16x32_bf16(__builtin_bit_cast(bf16x8, a), __builtin_bit_cast(bf16x8, b), c, 0, 0, 0);
}
DI f32x4 mfma16(v2u a, v2u b, f32x4 c) {
  return __builtin_amdgcn_mfma_f32_16x16x16bf16_1k(__builtin_bit_cast(s16x4, a), __builtin_bit_cast(s16x4, b), c, 0, 0, 0);
}
template <int CTRL> DI unsigned dppu(unsigned x) { return (unsigned)__builtin_amdgcn_mov_dpp((int)x, CTRL, 0xf, 0xf, true); }
template <int CTRL> DI float dpp(float x) { return __builtin_bit_cast(float, dppu<CTRL>(__builtin_bit_cast(unsigned, x))); }
constexpr int XOR1 = 0xB1, XOR2 = 0x4E, HMIR = 0x141, MIR = 0x140, ROR8 = 0x128;
DI unsigned pk_bf16(float lo, float hi) {
  f32x2 v = {lo, hi};
  return __builtin_bit_cast(unsigned, __builtin_convertvector(v, bf16x2));
}
DI float rbf(float x) { return __builtin_bit_cast(float, pk_bf16(x, x) & 0xffff0000u); }
DI float bf_lo(unsigned u) { return __builtin_bit_cast(float, u << 16); }
DI float bf_hi(unsigned u) { return __builtin_bit_cast(float, u & 0xffff0000u); }
DI float u2f(unsigned u) { return __builtin_bit_cast(float, u); }
DI unsigned f2u(float f) { return __builtin_bit_cast(unsigned, f); }
DI float xrow_max(float x) {
  auto s = __builtin_amdgcn_permlane16_swap(f2u(x), f2u(x), false, false);
  x = fmaxf(u2f(s[0]), u2f(s[1]));
  auto t = __builtin_amdgcn_permlane32_swap(f2u(x), f2u(x), false, false);
  return fmaxf(u2f(t[0]), u2f(t[1]));
}
DI float xrow_sum(float x) {
  auto s = __builtin_amdgcn_permlane16_swap(f2u(x), f2u(x), false, false);
  x = u2f(s[0]) + u2f(s[1]);
  auto t = __builtin_amdgcn_permlane32_swap(f2u(x), f2u(x), false, false);
  return u2f(t[0]) + u2f(t[1]);
}
DI float wave_sum(float x) {
  x += dpp<XOR1>(x); x += dpp<XOR2>(x); x += dpp<HMIR>(x); x += dpp<MIR>(x);
  return xrow_sum(x);
}
DI __amdgpu_buffer_rsrc_t rsrc(const void* p, unsigned bytes) {
  return __builtin_amdgcn_make_buffer_rsrc(const_cast<void*>(p), 0, bytes, 0x00020000);
}
template <int AUX> DI v4u ld128(__amdgpu_buffer_rsrc_t r, unsigned voff, unsigned soff) {
  return __builtin_amdgcn_raw_buffer_load_b128(r, voff, soff, AUX);
}
template <int AUX> DI v2u ld64(__amdgpu_buffer_rsrc_t r, unsigned voff, unsigned soff) {
  return __builtin_amdgcn_raw_buffer_load_b64(r, voff, soff, AUX);
}
DI void st128(__amdgpu_buffer_rsrc_t r, unsigned voff, unsigned soff, v4u v) { __builtin_amdgcn_raw_buffer_store_b128(v, r, voff, soff, SC1); }
DI void st64(__amdgpu_buffer_rsrc_t r, unsigned voff, unsigned soff, v2u v) { __builtin_amdgcn_raw_buffer_store_b64(v, r, voff, soff, SC1); }
DI v2u trread(LAS unsigned char* p) {
  return __builtin_bit_cast(v2u, __builtin_amdgcn_ds_read_tr16_b64_v4i16((LAS s16x4*)p));
}
DI bool tags_ok(v4u g, unsigned T) { return (g[1] == T) & (g[3] == T); }
DI void wg_barrier() {
  asm volatile("s_waitcnt lgkmcnt(0)" ::: "memory");
  __builtin_amdgcn_s_barrier();
  asm volatile("" ::: "memory");
}
template <int N> DI void wait_vm() { asm volatile("s_waitcnt vmcnt(%0)" ::"i"(N) : "memory"); }
DI unsigned clock_now() { return (unsigned)__builtin_amdgcn_s_memrealtime(); }
DI void hold(unsigned t) {
  asm volatile(
      "1:\n\ts_memrealtime vcc\n\ts_waitcnt lgkmcnt(0)\n\ts_sub_u32 vcc_lo, vcc_lo, %0\n\ts_cmp_lt_i32 vcc_lo, 0\n\t"
      "s_cbranch_scc0 2f\n\ts_sleep 1\n\ts_branch 1b\n2:"
      : : "s"(t) : "vcc", "scc", "memory");
}

DI v4u wfrag(LAS unsigned char* tile, int lane, int ks, int cq) {
  const int g = lane >> 4, q4 = (lane & 15) >> 2, p = lane & 3;
  LAS unsigned char* ta = tile + (32 * ks + 8 * g + q4) * TP_STR + 32 * cq + 8 * p;
  v2u a0 = trread(ta), a1 = trread(ta + 4 * TP_STR);
  return v4u{a0[0], a0[1], a1[0], a1[1]};
}
DI void slice_product(LAS unsigned char* stg, int lane, v4u b0, v4u b1) {
  f32x4 c[4];
#pragma unroll
  for (int cq = 0; cq < 4; ++cq) {
    c[cq] = mfma32(wfrag(stg, lane, 0, cq), b0, f32x4{0, 0, 0, 0});
    c[cq] = mfma32(wfrag(stg, lane, 1, cq), b1, c[cq]);
  }
#pragma unroll
  for (int cq = 0; cq < 4; ++cq) *(LAS f32x4*)(stg + cq * 1024 + lane * 16) = c[cq];
  asm volatile("s_waitcnt lgkmcnt(0)" ::: "memory");
}
template <typename V> DI V slices_sum(LAS unsigned char* lds, int off) {
  V c[8];
#pragma unroll
  for (int u = 0; u < 8; ++u) c[u] = *(LAS V*)(lds + L_STG + u * STG + off);
  return ((c[0] + c[1]) + (c[2] + c[3])) + ((c[4] + c[5]) + (c[6] + c[7]));
}
DI unsigned arrive(LAS unsigned char* cnt, int lane) {
  unsigned arr = 0;
  if (lane == 0) arr = __hip_atomic_fetch_add((LAS unsigned*)cnt, 1u, __ATOMIC_RELAXED, __HIP_MEMORY_SCOPE_WORKGROUP);
  arr = __builtin_amdgcn_readfirstlane(arr);
  asm volatile("" ::: "memory");
  return arr;
}
DI void wait_cnt(LAS unsigned char* cnt, unsigned n) {
  SPIN(CAP_L) {
    const unsigned c = __hip_atomic_load((LAS unsigned*)cnt, __ATOMIC_RELAXED, __HIP_MEMORY_SCOPE_WORKGROUP);
    asm volatile("" ::: "memory");
    if (__builtin_amdgcn_readfirstlane(c) == n) break;
    __builtin_amdgcn_s_sleep(1);
  }
}
template <int I> struct IC { static constexpr int v = I; };
template <int A, int B, typename F> DI void static_for(F&& f) {
  if constexpr (A < B) {
    f(IC<A>{});
    static_for<A + 1, B>(f);
  }
}
constexpr int pv_done(int i, int NS, int NR, int NG) {
  int done = 0;
  for (int k = NR + NG + 1; k <= i; ++k)
    for (int u = 0; u < 3; ++u)
      if (done <= k - NS - WIN) ++done;
  return done;
}

struct Args {
  const uint16_t *kc, *vc, *wq, *w1, *w2, *x, *b1;
  unsigned char* ws;
  const uint16_t *wo, *b2, *gw;
  uint16_t* out;
};
static_assert(sizeof(Args) == 96, "no padding");
struct Role {
  int grp, sp, tile;
  bool isQ, isH, isF;
};
struct Early {
  v4u pf, f4[4], lt[4], bf[2];
  v2u lb1[2];
  unsigned tch, E;
};

template <bool HEAVY>
DI void body(const Args& A, const Role& R, LAS unsigned char* lds, const Early& Y, unsigned long long t0) {
  constexpr int NS = HEAVY ? NSH : NSL, NR = HEAVY ? NRH : NRL, NG = (NS + 3) / 4, QPOS = NR - WIN - 2;
  constexpr int T4 = HEAVY ? TH0 : TL0, G1 = HEAVY ? G1H : G1L;
  const int j = blockIdx.x, tid = threadIdx.x, lane = tid & 63;
  const int w = __builtin_amdgcn_readfirstlane(tid >> 6);
  const int x8 = j & 7, r32 = j >> 3;
  const int sp = R.sp, b = R.grp >> 3, kvh = R.grp & 7;
  const bool isQ = HEAVY && R.isQ, isH = HEAVY && R.isH, isF = HEAVY && R.isF;
  const int fks = (R.tile - 28) >> 3, fns = (R.tile - 28) & 7;
  const int hd = x8 * 4 + (r32 >> 3), ns = (r32 + 6) & 7;
  const int fb = r32 >> 2, fq = r32 & 3;
  const __amdgpu_buffer_rsrc_t rws = rsrc(A.ws, WS_END);
  LAS unsigned char* cnt = lds + L_CNT;

  const __amdgpu_buffer_rsrc_t rk = rsrc(A.kc + (size_t)R.grp * 4096 * 128, 1 << 20), rv = rsrc(A.vc + (size_t)R.grp * 4096 * 128, 1 << 20);
  const unsigned qsel = ((lane & 15) >> 2) * 256 + (lane >> 4) * 64 + (lane & 3) * 16;
  const unsigned sbase = (((sp >> 1) * 64 + (HEAVY ? 0 : NSH)) * 8 + w) * 1024;
  const unsigned koff = qsel + sbase, voff = lane * 16 + sbase;
  v4u kv[NR];
  unsigned tb = (unsigned)t0, tf = 0;
  auto sched = [](int i) constexpr { return T4 + (TEND - T4) * (i - 4) / (2 * NS - 5); };
  auto slot_load = [&](int i) INL {
    if (i >= 4 && i % 2 == 0) {
      if ((i > G1 + WIN && i <= G1 + WIN + 6) || (i >= NR && i < NR + 2)) tb = __builtin_amdgcn_readfirstlane(*(LAS unsigned*)(cnt + C_ORG));
      hold(tb + sched(i));
    }
    kv[i % NR] = i < NS ? ld128<NT>(rk, koff, i * 8192) : ld128<NT>(rv, voff, (i - NS) * 8192);
  };
  LAS unsigned char* stg = lds + L_STG + w * STG;
  unsigned E = Y.E, T;
  auto tag = [&]() INL {
    asm volatile("s_waitcnt lgkmcnt(0)" : "+s"(E) : : "memory");
    T = E * 1664525u + 1013904223u;
  };
  if (tid < 10) *(LAS unsigned*)(cnt + tid * 4) = tid == C_ORG / 4 ? tb : 0;
  wg_barrier();
  if (!HEAVY) {
#pragma unroll
    for (int i = 0; i < 4; ++i) kv[i] = Y.f4[i];
    wait_vm<4>();
    tf = clock_now();
  } else {
#pragma unroll
    for (int i = 0; i < 4; ++i) {
      wait_vm<8>();
      slot_load(i);
      SCHED_FENCE();
    }
    tag();
#pragma unroll
    for (int i = 0; i < 8; ++i) *(LAS v4u*)(stg + (8 * i + (lane >> 3)) * TP_STR + (lane & 7) * 16) = i < 4 ? Y.f4[i] : Y.lt[i - 4];
    if (isQ | isH) {
      slice_product(stg, lane, Y.bf[0], Y.bf[1]);
      if (arrive(cnt + C_TILE, lane) == 0) {
        wait_cnt(cnt + C_TILE, 8);
#pragma unroll
        for (int u = 0; u < 2; ++u) {
          const int cq = 2 * u + ((lane >> 3) & 1);
          f32x4 c = slices_sum<f32x4>(lds, cq * 1024 + (lane & 0x37) * 16);
          if (isH) {
            c[0] = rbf(rbf(c[0]) + bf_lo(Y.lb1[u][0])); c[1] = rbf(rbf(c[1]) + bf_hi(Y.lb1[u][0]));
            c[2] = rbf(rbf(c[2]) + bf_lo(Y.lb1[u][1])); c[3] = rbf(rbf(c[3]) + bf_hi(Y.lb1[u][1]));
#pragma unroll
            for (int r = 0; r < 4; ++r) { c[r] = c[r] > 0.f ? c[r] : 0.f; c[r] = c[r] * c[r]; }
          }
          st128(rws, isQ ? (lane & 7) * 16384 + cq * 64 + (lane >> 4) * 16 : cq * 512 + (lane >> 4) * 128 + (lane & 7) * 16,
                isQ ? WS_QG + R.tile * 256 : WS_HG + R.tile * 2048, v4u{pk_bf16(c[0], c[1]), T, pk_bf16(c[2], c[3]), T});
        }
      }
    }
    asm volatile("" ::"v"(Y.pf), "v"(Y.tch));
  }
  SCHED_FENCE();

  float gate = 0.f;
  v4u qg[2], hg[2], lgx, lgw, tg;
  unsigned lb2;
  bool lead;
  auto rep_load = [&]() INL { tg = ld128<SC1>(rws, lead ? lane * 16 : OOB, WS_T0); };
  auto rep_mean = [&](int late) INL {
    float e[2];
#pragma unroll
    for (int u = 0; u < 2; ++u) e[u] = (float)(tg[2 * u + 1] == T ? min(max((int)(tg[2 * u] - (unsigned)t0), -1000), 1000) : late);
    float d = wave_sum(e[0] + e[1]) * (1.f / 128.f);
    d += wave_sum(fabsf(e[0] - d) + fabsf(e[1] - d)) * (0.5f / 128.f);
    *(LAS unsigned*)(cnt + C_ORG) = (unsigned)t0 + (int)d + OB;
  };
  const bool hpad = lane >= 48;
  auto qh_load = [&]() INL {
#pragma unroll
    for (int u = 0; u < 2; ++u) qg[u] = ld128<SC1>(rws, lead ? lane * 16 + u * 1024 : OOB, WS_QG + (b * 2048 + kvh * 256) * 8);
    if (HEAVY) {
      const unsigned so = isF ? WS_HG + (8 * fks + w) * 1792 : 0;
      hg[0] = ld128<SC1>(rws, isF ? lane * 16 : OOB, so);
      hg[1] = ld128<SC1>(rws, (isF && !hpad) ? lane * 16 + 1024 : OOB, so);
    }
  };
  auto qh_ok = [&]() INL {
    bool ok = !lead | (tags_ok(qg[0], T) & tags_ok(qg[1], T));
    if (HEAVY) ok &= !isF | (tags_ok(hg[0], T) & (hpad | tags_ok(hg[1], T)));
    return __all(ok);
  };
  static_assert(NS <= NR && G1 + WIN <= GPOS && (HEAVY || 5 + WIN <= G1) && GPOS + WIN <= QPOS, "");
  static_for<4, NR>([&](auto ic) INL {
    constexpr int i = decltype(ic)::v;
    if (i == 5 && !HEAVY) {
      tag();
      const bool me = arrive(cnt + C_REP, lane) == 0;
      st64(rws, (me && lane == 0) ? 0 : OOB, WS_T0 + (j >> 1) * 8, v2u{(int)(tf - AL0 - tb) > 0 ? tf - AL0 : tb, T});
      SCHED_FENCE();
    }
    if (i == WIN + 2 && !HEAVY) asm volatile("" ::"v"(Y.pf), "v"(Y.tch));
    if (i == G1) {
      lead = arrive(cnt + C_RLEAD, lane) == 0;
      rep_load();
      SCHED_FENCE();
    }
    if (i == G1 + WIN) {
      if (lead) rep_mean(sched(G1) - 250);
      SCHED_FENCE();
    }
    if (i == GPOS) {
      lgx = ld128<0>(rsrc(A.x, 8192), w == 4 ? lane * 16 : OOB, fb * 1024);
      lgw = ld128<0>(rsrc(A.gw, 1024), w == 4 ? lane * 16 : OOB, 0);
      lb2 = __builtin_amdgcn_raw_buffer_load_b32(rsrc(A.b2, 1024), w == 4 ? (lane & 7) * 4 : OOB, (64 * x8 + 16 * fq) * 2, 0);
      SCHED_FENCE();
    }
    if (i == GPOS + WIN) {
      if (w == 4) {
        float t = 0.f;
#pragma unroll
        for (int u = 0; u < 4; ++u) t += rbf(bf_lo(lgx[u]) * bf_lo(lgw[u])) + rbf(bf_hi(lgx[u]) * bf_hi(lgw[u]));
        t = rbf(wave_sum(t));
        gate = rbf(1.f / (1.f + __expf(-t)));
      }
      asm volatile("" : "+v"(gate));
      SCHED_FENCE();
    }
    if (i == QPOS) {
      lead = arrive(cnt + C_QLEAD, lane) == 0;
      qh_load();
      rep_load();
      SCHED_FENCE();
    }
    if (i == QPOS + WIN) {
      if (!qh_ok()) {
        SPIN(CAP_G) {
          __builtin_amdgcn_s_sleep(2);
          qh_load();
          const bool ok = qh_ok();
          asm volatile("" ::: "memory");
          if (ok) break;
        }
      }
      if (lead) {
#pragma unroll
        for (int u = 0; u < 2; ++u) *(LAS v2u*)(lds + L_QS + u * 512 + lane * 8) = v2u{qg[u][0], qg[u][2]};
        rep_mean(sched(QPOS) - 250);
        asm volatile("s_waitcnt lgkmcnt(0)" ::: "memory");
        __hip_atomic_store((LAS unsigned*)(cnt + C_QRDY), 1u, __ATOMIC_RELAXED, __HIP_MEMORY_SCOPE_WORKGROUP);
      }
      if (HEAVY && isF) {
        v4u hf[2];
        const bool hi = lane & 8;
#pragma unroll
        for (int s = 0; s < 2; ++s) {
          const unsigned o0 = dppu<ROR8>(hg[s][0]), o1 = dppu<ROR8>(hg[s][2]);
          hf[s] = hi ? v4u{o0, o1, hg[s][0], hg[s][2]} : v4u{hg[s][0], hg[s][2], o0, o1};
        }
        slice_product(stg, lane, hf[0], hf[1]);
        arrive(cnt + C_FFN, lane);
      }
      SCHED_FENCE();
    }
    if (i >= QPOS && i < QPOS + WIN) wait_vm<WIN - 1 + (HEAVY ? 5 : 3)>();
    else if (i >= GPOS && i < GPOS + WIN) wait_vm<WIN - 1 + 3>();
    else if ((!HEAVY && i >= 5 && i < 5 + WIN) || (i >= G1 && i < G1 + WIN)) wait_vm<WIN>();
    else wait_vm<WIN - 1>();
    slot_load(i);
    SCHED_FENCE();
  });
  wait_cnt(cnt + C_QRDY, 1);
  const v4u qf = *(LAS v4u*)(lds + L_QS + qsel);
  SCHED_FENCE();

  const int cc = lane & 3;
  const unsigned mask0 = cc == 0 ? 0x0000ffffu : cc == 1 ? 0xffff0000u : 0u;
  const unsigned mask1 = cc == 2 ? 0x0000ffffu : cc == 3 ? 0xffff0000u : 0u;
  constexpr float C2 = 0.08838834764831845f * 1.4426950408889634f;
  float S[NG];
  unsigned pp[NG];
  f32x4 a0 = {0, 0, 0, 0}, a1 = {0, 0, 0, 0};
  auto score = [&](v4u k) INL {
    const f32x4 d = mfma32(k, qf, f32x4{0, 0, 0, 0});
    return (dpp<0x00>(d[0]) + dpp<0x55>(d[1])) + (dpp<0xAA>(d[2]) + dpp<0xFF>(d[3]));
  };
  auto pv = [&](int i) INL {
    const unsigned pb = (i & 3) == 0 ? dppu<0x00>(pp[i >> 2]) : (i & 3) == 1 ? dppu<0x55>(pp[i >> 2]) : (i & 3) == 2 ? dppu<0xAA>(pp[i >> 2]) : dppu<0xFF>(pp[i >> 2]);
    const v2u pa = {pb & mask0, pb & mask1};
    const v4u v = kv[(NS + i) % NR];
    a0 = mfma16(pa, v2u{v[0], v[1]}, a0);
    a1 = mfma16(pa, v2u{v[2], v[3]}, a1);
  };
  const unsigned sf = WS_FG + (x8 * 32 + r32) * 512;
  v4u fg, lo[2];
  float f0 = 0.f, f1 = 0.f;
  bool fok = false;
  auto ffn_part = [&]() INL {
    f0 = lane < 32 ? u2f(fg[0]) : 0.f;
    f1 = lane < 32 ? u2f(fg[2]) : 0.f;
    f0 += dpp<ROR8>(f0); f1 += dpp<ROR8>(f1);
    f0 = xrow_sum(f0); f1 = xrow_sum(f1);
    const float og = rbf(1.f - gate);
    f0 = rbf(og * rbf(rbf(f0) + bf_lo(lb2)));
    f1 = rbf(og * rbf(rbf(f1) + bf_hi(lb2)));
    asm volatile("" : "+v"(f0), "+v"(f1));
  };
  constexpr int FS = NR + 3, FP = 2 * NS - 10 - WIN;
  static_assert(2 * NS - NR >= NG + 2 + WIN && HP <= WIN && FS + WIN + 4 <= FP, "");
  static_for<NR, 2 * NS>([&](auto ic) INL {
    constexpr int i = decltype(ic)::v, t = i - NR;
    if constexpr (t < NG) {
      const float s0 = score(kv[4 * t]), s1 = 4 * t + 1 < NS ? score(kv[(4 * t + 1) % NR]) : -INFINITY;
      const float s2 = 4 * t + 2 < NS ? score(kv[(4 * t + 2) % NR]) : -INFINITY, s3 = 4 * t + 3 < NS ? score(kv[(4 * t + 3) % NR]) : -INFINITY;
      S[t] = (cc & 2) ? ((cc & 1) ? s3 : s2) : ((cc & 1) ? s1 : s0);
    }
    if constexpr (t == NG) {
      float m = S[0];
#pragma unroll
      for (int u = 1; u < NG; ++u) m = fmaxf(m, S[u]);
      m = fmaxf(m, dpp<XOR1>(m));
      m = fmaxf(m, dpp<XOR2>(m));
      m = xrow_max(m) * C2;
      float l = 0.f;
#pragma unroll
      for (int u = 0; u < NG; ++u) {
        const float p = __builtin_amdgcn_exp2f(__builtin_fmaf(S[u], C2, -m));
        l += p;
        pp[u] = pk_bf16(p, p);
      }
      l += dpp<XOR1>(l);
      l += dpp<XOR2>(l);
      l = xrow_sum(l);
      if ((lane & 0x33) == 0) {
        *(LAS float*)(lds + L_ML + (lane >> 2) * 32 + w * 4) = m;
        *(LAS float*)(lds + L_ML + 128 + (lane >> 2) * 32 + w * 4) = l;
      }
      asm volatile("s_waitcnt lgkmcnt(0)" ::: "memory");
      if (arrive(cnt + C_SOFT, lane) == 7) {
        const float mw = *(LAS float*)(lds + L_ML + (lane & 31) * 4), lw = *(LAS float*)(lds + L_ML + 128 + (lane & 31) * 4);
        float M = fmaxf(mw, dpp<XOR1>(mw));
        M = fmaxf(M, dpp<XOR2>(M));
        M = fmaxf(M, dpp<HMIR>(M));
        const float ex = __builtin_amdgcn_exp2f(mw - M);
        float L = ex * lw;
        L += dpp<XOR1>(L);
        L += dpp<XOR2>(L);
        L += dpp<HMIR>(L);
        *(LAS float*)(lds + L_ML + 256 + (lane & 31) * 4) = ex * __builtin_amdgcn_rcpf(L);
        *(LAS float*)(lds + L_ML + 384 + ((lane >> 3) & 3) * 4) = M + __builtin_amdgcn_logf(L);
        asm volatile("s_waitcnt lgkmcnt(0)" ::: "memory");
      }
    }
    if constexpr (HEAVY && i == FS) {
      f32x2 c = {0, 0};
      if (isF && w < 4) {
        wait_cnt(cnt + C_FFN, 8);
        c = slices_sum<f32x2>(lds, w * 1024 + (lane & 0x37) * 16 + (lane & 8));
      }
      st128(rws, (isF && w < 4) ? (lane & 7) * 2048 + (lane >> 4) * 32 + (lane & 8) * 2 : OOB,
            isF ? WS_FG + ((fns * 32 + (w & 3)) * 64 + fks * 16) * 8 : 0, v4u{f2u(c[0]), T, f2u(c[1]), T});
    }
    if constexpr (i == FP) fg = ld128<SC1>(rws, w == 4 ? (lane & 31) * 16 : OOB, sf);
    if constexpr (i == FP + WIN) {
      if (w == 4) {
        fok = __all(tags_ok(fg, T));
        ffn_part();
      }
    }
    SCHED_FENCE();
    if constexpr ((i >= FP && i < FP + WIN) || (HEAVY && i >= FS && i < FS + WIN)) wait_vm<WIN>();
    else wait_vm<WIN - 1>();
    slot_load(i);
    SCHED_FENCE();
    static_for<pv_done(i - 1, NS, NR, NG), pv_done(i, NS, NR, NG)>([&](auto uc) INL { pv(decltype(uc)::v); });
    SCHED_FENCE();
  });
  static_for<pv_done(2 * NS - 1, NS, NR, NG), NS>([&](auto uc) INL {
    constexpr int u = decltype(uc)::v;
    if constexpr (u == NS - HP) {
      const bool last = arrive(cnt + C_ANN1, lane) == 7;
      const unsigned lse = *(LAS unsigned*)(lds + L_ML + 384 + (lane & 3) * 4);
      st64(rws, (last && lane < 4) ? lane * 8 : OOB, WS_LG + ((kvh * 8 + b) * 4 + sp) * 128, v2u{lse, T});
      SCHED_FENCE();
    }
    pv(u);
    SCHED_FENCE();
  });
  wait_vm<1>();
#pragma unroll
  for (int u = 0; u < 2; ++u)
    lo[u] = ld128<NT>(rsrc(A.wo, 4096 * 1024), (lane >> 3) * 1024 + (lane & 7) * 16, (128 * hd + 16 * w + 8 * u) * 1024 + ns * 128);
  SCHED_FENCE();
  *(LAS f32x4*)(lds + L_MRG + w * 2048 + lane * 32) = a0;
  *(LAS f32x4*)(lds + L_MRG + w * 2048 + lane * 32 + 16) = a1;
  wg_barrier();

  if (w < 4) {
    const f32x4 g0 = *(LAS f32x4*)(lds + L_ML + 256 + w * 32), g1 = *(LAS f32x4*)(lds + L_ML + 256 + w * 32 + 16);
    f32x2 ov[8];
#pragma unroll
    for (int u = 0; u < 8; ++u) ov[u] = *(LAS f32x2*)(lds + L_MRG + u * 2048 + w * 512 + lane * 8);
    const f32x2 o = ((ov[0] * g0[0] + ov[1] * g0[1]) + (ov[2] * g0[2] + ov[3] * g0[3])) + ((ov[4] * g1[0] + ov[5] * g1[1]) + (ov[6] * g1[2] + ov[7] * g1[3]));
    const unsigned pk = pk_bf16(o[0], o[1]), nb = dppu<XOR1>(pk);
    st128(rws, (lane & 1) ? OOB : lane * 8, WS_OG + (((4 * kvh + w) * 8 + b) * 4 + sp) * 512, v4u{pk, T, nb, T});
  }
  SCHED_FENCE();

  v4u wf[4];
  {
    const unsigned so = WS_OG + (hd * 8 + w) * 2048, sl = WS_LG + ((hd >> 2) * 8 + w) * 512 + (hd & 3) * 8;
    v4u og[2];
    v2u lg;
    SPIN(CAP_G) {
      lg = ld64<SC1>(rws, cc * 128, sl);
      const bool ok = __all(lg[1] == T);
      asm volatile("" ::: "memory");
      if (ok) break;
    }
#pragma unroll
    for (int s = 0; s < 2; ++s) og[s] = ld128<SC1>(rws, lane * 16 + s * 1024, so);
    SCHED_FENCE();
    {
      const unsigned now = clock_now();
      st64(rws, (arrive(cnt + C_ANN2, lane) == 7 && lane == 0) ? 0 : OOB, WS_H2 + (ns * 32 + hd) * 128, v2u{now, T});
    }
    const float ls = u2f(lg[0]);
    float M = fmaxf(ls, dpp<XOR1>(ls));
    M = fmaxf(M, dpp<XOR2>(M));
    const float ex = __builtin_amdgcn_exp2f(ls - M);
    float Z = ex + dpp<XOR1>(ex);
    Z += dpp<XOR2>(Z);
    const float wn = ex * __builtin_amdgcn_rcpf(Z);
    const float m0 = dpp<0x00>(wn), m1 = dpp<0x55>(wn), m2 = dpp<0xAA>(wn), m3 = dpp<0xFF>(wn);
    float w0 = lane < 32 ? m0 : m1, w1 = lane < 32 ? m2 : m3;
    asm volatile("" : "+v"(w0), "+v"(w1));
#pragma unroll
    for (int u = 0; u < 2; ++u) *(LAS v4u*)(lds + L_TO + (16 * w + 8 * u + (lane >> 3)) * TP_STR + (lane & 7) * 16) = lo[u];
    wg_barrier();
#pragma unroll
    for (int s = 0; s < 4; ++s) wf[s] = wfrag(lds + L_TO, lane, s, w & 3);
    SCHED_FENCE();
    if (!__all(tags_ok(og[0], T) & tags_ok(og[1], T))) {
      SPIN(CAP_G) {
#pragma unroll
        for (int s = 0; s < 2; ++s) og[s] = ld128<SC1>(rws, lane * 16 + s * 1024, so);
        const bool ok = __all(tags_ok(og[0], T) & tags_ok(og[1], T));
        asm volatile("" ::: "memory");
        if (ok) break;
      }
    }
    const float o0 = w0 * bf_lo(og[0][0]) + w1 * bf_lo(og[1][0]), o1 = w0 * bf_hi(og[0][0]) + w1 * bf_hi(og[1][0]);
    const float o2 = w0 * bf_lo(og[0][2]) + w1 * bf_lo(og[1][2]), o3 = w0 * bf_hi(og[0][2]) + w1 * bf_hi(og[1][2]);
    auto x0 = __builtin_amdgcn_permlane32_swap(f2u(o0), f2u(o2), false, false);
    auto x1 = __builtin_amdgcn_permlane32_swap(f2u(o1), f2u(o3), false, false);
    *(LAS unsigned*)(lds + L_OS + w * OS_STR + (lane & 31) * 8 + (lane >> 5) * 4) = pk_bf16(u2f(x0[0]) + u2f(x0[1]), u2f(x1[0]) + u2f(x1[1]));
  }
  wg_barrier();

  if (w < 4) {
    v4u ob[4];
#pragma unroll
    for (int s = 0; s < 4; ++s) ob[s] = *(LAS v4u*)(lds + L_OS + (lane & 7) * OS_STR + s * 64 + (lane >> 4) * 16);
    f32x4 c = mfma32(wf[0], ob[0], f32x4{0, 0, 0, 0}), c2 = mfma32(wf[1], ob[1], f32x4{0, 0, 0, 0});
    c = mfma32(wf[2], ob[2], c);
    c2 = mfma32(wf[3], ob[3], c2);
    c += c2;
    const bool hi = lane & 8;
    st128(rws, (lane & 7) * 16384 + (lane >> 4) * 32 + (lane & 8) * 2, WS_PG + ((ns * 32 + w) * 512 + hd * 16) * 8,
          v4u{f2u(hi ? c[2] : c[0]), T, f2u(hi ? c[3] : c[1]), T});
  } else if (w == 4) {
    const unsigned sp_ = WS_PG + (x8 * 32 + r32) * 4096;
    if (!fok) {
      SPIN(CAP_G) {
        fg = ld128<SC1>(rws, (lane & 31) * 16, sf);
        const bool ok = __all(tags_ok(fg, T));
        asm volatile("" ::: "memory");
        if (ok) break;
      }
      ffn_part();
    }
    v2u hn;
    SPIN(CAP_G) {
      hn = ld64<SC1>(rws, (lane & 31) * 128, WS_H2 + x8 * 4096);
      asm volatile("" ::: "memory");
      if (__all(hn[1] == T)) break;
    }
    {
      int d = (int)(hn[0] - tb);
      d = max(d, (int)dppu<XOR1>(d));
      d = max(d, (int)dppu<XOR2>(d));
      d = max(d, (int)dppu<HMIR>(d));
      d = max(d, (int)dppu<MIR>(d));
      hold(tb + min(max(__builtin_amdgcn_readlane(d, 0), __builtin_amdgcn_readlane(d, 16)), 10000) + C2L);
    }
    v4u pg[4];
    SPIN(CAP_G) {
#pragma unroll
      for (int i = 0; i < 4; ++i) pg[i] = ld128<SC1>(rws, lane * 16 + i * 1024, sp_);
      const bool ok = __all(tags_ok(pg[0], T) & tags_ok(pg[1], T) & tags_ok(pg[2], T) & tags_ok(pg[3], T));
      asm volatile("" ::: "memory");
      if (ok) break;
    }
    float s0 = (u2f(pg[0][0]) + u2f(pg[1][0])) + (u2f(pg[2][0]) + u2f(pg[3][0]));
    float s1 = (u2f(pg[0][2]) + u2f(pg[1][2])) + (u2f(pg[2][2]) + u2f(pg[3][2]));
    s0 += dpp<ROR8>(s0); s1 += dpp<ROR8>(s1);
    s0 = xrow_sum(s0); s1 = xrow_sum(s1);
    const float r0 = rbf(gate * rbf(s0)) + f0;
    const float r1 = rbf(gate * rbf(s1)) + f1;
    __builtin_amdgcn_raw_buffer_store_b32(pk_bf16(r0, r1), rsrc(A.out, 8192), lane < 8 ? lane * 4 : OOB, (fb * 512 + 64 * x8 + 16 * fq) * 2, SC1);
    __builtin_amdgcn_raw_buffer_store_b32(T, rws, (j == 0 && lane == 0) ? 0 : OOB, 0, SC1);
  }
}

__global__ void __launch_bounds__(512, 2) fused_decode(Args) {
  __shared__ __attribute__((aligned(16))) unsigned char lds_raw[L_END];
  unsigned long long t0;
  u64x8 ka;
  u64x4 kb;
  asm volatile("s_load_dwordx16 %0, %3, 0x0\n\ts_load_dwordx8 %1, %3, 0x40\n\ts_memrealtime %2"
               : "=&s"(ka), "=&s"(kb), "=&s"(t0) : "s"(__builtin_amdgcn_kernarg_segment_ptr()));
  SCHED_FENCE();
  unsigned long long pc;
  asm volatile("s_getpc_b64 %0" : "=s"(pc));
  Early Y;
  Y.pf = ld128<0>(rsrc((const void*)(pc & ~255ull), CODEB), blockIdx.x < 32 ? threadIdx.x * 16 + (blockIdx.x >> 3) * 8192 : OOB, 0);
  SCHED_FENCE();
  const int j = blockIdx.x, lane = threadIdx.x & 63, w = __builtin_amdgcn_readfirstlane(threadIdx.x >> 6);
  const int x8 = j & 7, r32 = j >> 3;
  Role R;
  R.grp = x8 * 8 + (r32 >> 2);
  R.sp = (r32 + x8) & 3;
  R.tile = (r32 >> 2) * 8 + ((x8 + (r32 >> 2)) & 7);
  const bool heavy = (R.sp & 1) == 0;
  R.isQ = R.sp == 0;
  R.isH = R.sp == 2 && R.tile < 28;
  R.isF = R.sp == 2 && R.tile >= 28 && R.tile < 60;
  unsigned oK = (R.grp << 20) + (((R.sp >> 1) * 64 + NSH) * 8 + w) * 1024, oQ = w * (64 * 8192) + R.tile * 128;
  unsigned oH = w * (64 * 3584) + R.tile * 128, oF = (448 * ((R.tile - 28) >> 3) + 56 * w) * 1024 + ((R.tile - 28) & 7) * 128;
  asm volatile("" : "+s"(oK), "+s"(oQ), "+s"(oH), "+s"(oF));
  const bool off = heavy && !(R.isQ | R.isH | R.isF);
  unsigned rs = off ? 0 : R.isQ ? 8192 : R.isH ? 3584 : 1024;
  unsigned bytes = off ? 0 : !heavy ? 1 << 26 : R.isQ ? 512 * 8192 : 1792 * 1024;
  unsigned org = !heavy ? oK : R.isQ ? oQ : R.isH ? oH : oF;
  asm volatile("" : "+s"(rs), "+s"(bytes), "+s"(org));
  unsigned lk = ((lane & 15) >> 2) * 256 + (lane >> 4) * 64 + (lane & 3) * 16, lw = (lane >> 3) * rs + (lane & 7) * 16;
  asm volatile("" : "+v"(lk), "+v"(lw));
  unsigned vo = org + (heavy ? lw : lk);
  asm volatile("" : "+v"(vo));
  SCHED_FENCE();
  asm volatile("s_waitcnt lgkmcnt(0)" : "+s"(ka), "+s"(kb), "+s"(t0));
  Args A;
  A.kc = (const uint16_t*)ka[0]; A.vc = (const uint16_t*)ka[1]; A.wq = (const uint16_t*)ka[2]; A.w1 = (const uint16_t*)ka[3];
  A.w2 = (const uint16_t*)ka[4]; A.x = (const uint16_t*)ka[5]; A.b1 = (const uint16_t*)ka[6]; A.ws = (unsigned char*)ka[7];
  A.wo = (const uint16_t*)kb[0]; A.b2 = (const uint16_t*)kb[1]; A.gw = (const uint16_t*)kb[2]; A.out = (uint16_t*)kb[3];
  asm volatile("s_load_dword %0, %1, 0x0" : "=s"(Y.E) : "s"(A.ws) : "memory");
  const __amdgpu_buffer_rsrc_t r0 = rsrc(!heavy ? A.kc : R.isQ ? A.wq : R.isH ? A.w1 : A.w2, bytes);
#pragma unroll
  for (int i = 0; i < 4; ++i) Y.f4[i] = ld128<NT>(r0, vo, 8 * i * rs);
  SCHED_FENCE();
  if (heavy) {
#pragma unroll
    for (int i = 0; i < 4; ++i) Y.lt[i] = ld128<NT>(r0, (i == 3 && R.isF) ? OOB : vo, 8 * (i + 4) * rs);
#pragma unroll
    for (int s = 0; s < 2; ++s) Y.bf[s] = ld128<0>(rsrc(A.x, 8192), (lane & 7) * 1024 + (lane >> 4) * 16 + s * 64, w * 128);
#pragma unroll
    for (int u = 0; u < 2; ++u) Y.lb1[u] = ld64<0>(rsrc(A.b1, 1792 * 2), R.isH ? ((lane >> 3) & 1) * 32 + (lane >> 4) * 8 + u * 64 : OOB, R.isH ? R.tile * 128 : 0);
  }
  SCHED_FENCE();
  {
    unsigned long long c0 = ka[1] + oK, c1 = ka[0] + oK, c2 = kb[0] + (x8 * 4 + (r32 >> 3)) * 131072, c3 = ka[7] + 1024;
    asm volatile("" : "+s"(c0), "+s"(c1), "+s"(c2), "+s"(c3));
    unsigned long long d0 = (w & 1) ? c1 : c0, d1 = (w & 1) ? c3 : c2, d2 = (w & 1) ? kb[3] : ka[5], d3 = (w & 1) ? kb[1] : kb[2];
    asm volatile("" : "+s"(d0), "+s"(d1), "+s"(d2), "+s"(d3));
    d0 = (w & 2) ? d1 : d0;
    d2 = (w & 2) ? d3 : d2;
    asm volatile("" : "+s"(d0), "+s"(d2));
    Y.tch = __builtin_amdgcn_raw_buffer_load_b32(rsrc((const void*)((w & 4) ? d2 : d0), 4), lane * 4, 0, NT);
  }
  SCHED_FENCE();
  if (__builtin_expect(heavy, 0)) body<true>(A, R, (LAS unsigned char*)lds_raw, Y, t0);
  else body<false>(A, R, (LAS unsigned char*)lds_raw, Y, t0);
}

extern "C" void kernel_launch(void* const* d_in, const int* in_sizes, int n_in, void* d_out, int out_size, void* d_ws, size_t ws_size,
                              hipStream_t stream) {
  Args A;
  A.x = (const uint16_t*)d_in[0]; A.kc = (const uint16_t*)d_in[1]; A.vc = (const uint16_t*)d_in[2];
  A.wq = (const uint16_t*)d_in[3]; A.wo = (const uint16_t*)d_in[4]; A.w1 = (const uint16_t*)d_in[6];
  A.b1 = (const uint16_t*)d_in[7]; A.w2 = (const uint16_t*)d_in[8]; A.b2 = (const uint16_t*)d_in[9];
  A.gw = (const uint16_t*)d_in[10]; A.out = (uint16_t*)d_out; A.ws = (unsigned char*)d_ws;
  fused_decode<<<256, 512, 0, stream>>>(A);
}
```

```cpp
#include <hip/hip_runtime.h>
#include <hip/hip_bf16.h>
#include <stdint.h>

constexpr int WIN = 5;
constexpr int NSH = 30, NSL = 64 - NSH;
constexpr int NRH = 30, NRL = 36;
constexpr int G1H = 4, G1L = 12;
constexpr int GPOS = 18;
constexpr int HP = 3;
constexpr int TEND = 1876;
constexpr int TH0 = 320, TL0 = 130;
constexpr int AL0 = 105;
constexpr int OB = 36;
constexpr int C2L = 109;
constexpr int CODEB = 28672;

typedef unsigned v4u __attribute__((ext_vector_type(4)));
typedef unsigned v2u __attribute__((ext_vector_type(2)));
typedef float f32x4 __attribute__((ext_vector_type(4)));
typedef float f32x2 __attribute__((ext_vector_type(2)));
typedef short s16x4 __attribute__((ext_vector_type(4)));
typedef __bf16 bf16x8 __attribute__((ext_vector_type(8)));
typedef __bf16 bf16x2 __attribute__((ext_vector_type(2)));
typedef unsigned long long u64x8 __attribute__((ext_vector_type(8)));
typedef unsigned long long u64x4 __attribute__((ext_vector_type(4)));
#define LAS __attribute__((address_space(3)))
#define DI __device__ __forceinline__
#define INL __attribute__((always_inline))
#define SCHED_FENCE() __builtin_amdgcn_sched_barrier(0)
#define SPIN(cap) _Pragma("nounroll") for (unsigned spin = 0; spin < (cap); ++spin)

constexpr int NT = 2, SC1 = 16;
constexpr unsigned OOB = 0x7fffff00u;
constexpr unsigned CAP_G = 1u << 21, CAP_L = 1u << 25;
constexpr unsigned WS_T0 = 4096;
constexpr unsigned WS_QG = 8192;
constexpr unsigned WS_HG = WS_QG + 8 * 2048 * 8;
constexpr unsigned WS_OG = WS_HG + 65536;
constexpr unsigned WS_LG = WS_OG + 32 * 32 * 64 * 8;
constexpr unsigned WS_PG = WS_LG + 256 * 128;
constexpr unsigned WS_FG = WS_PG + 256 * 32 * 16 * 8;
constexpr unsigned WS_H2 = WS_FG + 256 * 4 * 16 * 8;
constexpr unsigned WS_END = WS_H2 + 256 * 128;
constexpr int TP_STR = 160, OS_STR = 272, STG = 64 * TP_STR;
constexpr int L_STG = 0;
constexpr int L_TO = 8 * STG;
constexpr int L_MRG = L_TO + 128 * TP_STR;
constexpr int L_OS = L_MRG + 8 * 2048;
constexpr int L_QS = L_OS + 8 * OS_STR;
constexpr int L_ML = L_QS + 1024;
constexpr int L_CNT = L_ML + 400;
constexpr int L_END = L_CNT + 64;
enum { C_TILE = 0, C_ANN1 = 4, C_ANN2 = 8, C_QRDY = 12, C_FFN = 16, C_QLEAD = 20, C_SOFT = 24, C_REP = 28, C_RLEAD = 32, C_ORG = 36 };

DI f32x4 mfma32(v4u a, v4u b, f32x4 c) {
  return __builtin_amdgcn_mfma_f32_16x16x32_bf16(__builtin_bit_cast(bf16x8, a), __builtin_bit_cast(bf16x8, b), c, 0, 0, 0);
}
DI f32x4 mfma16(v2u a, v2u b, f32x4 c) {
  return __builtin_amdgcn_mfma_f32_16x16x16bf16_1k(__builtin_bit_cast(s16x4, a), __builtin_bit_cast(s16x4, b), c, 0, 0, 0);
}
template <int CTRL> DI unsigned dppu(unsigned x) { return (unsigned)__builtin_amdgcn_mov_dpp((int)x, CTRL, 0xf, 0xf, true); }
template <int CTRL> DI float dpp(float x) { return __builtin_bit_cast(float, dppu<CTRL>(__builtin_bit_cast(unsigned, x))); }
constexpr int XOR1 = 0xB1, XOR2 = 0x4E, HMIR = 0x141, MIR = 0x140, ROR8 = 0x128;
DI unsigned pk_bf16(float lo, float hi) {
  f32x2 v = {lo, hi};
  return __builtin_bit_cast(unsigned, __builtin_convertvector(v, bf16x2));
}
DI float rbf(float x) { return __builtin_bit_cast(float, pk_bf16(x, x) & 0xffff0000u); }
DI float bf_lo(unsigned u) { return __builtin_bit_cast(float, u << 16); }
DI float bf_hi(unsigned u) { return __builtin_bit_cast(float, u & 0xffff0000u); }
DI float u2f(unsigned u) { return __builtin_bit_cast(float, u); }
DI unsigned f2u(float f) { return __builtin_bit_cast(unsigned, f); }
DI float xrow_max(float x) {
  auto s = __builtin_amdgcn_permlane16_swap(f2u(x), f2u(x), false, false);
  x = fmaxf(u2f(s[0]), u2f(s[1]));
  auto t = __builtin_amdgcn_permlane32_swap(f2u(x), f2u(x), false, false);
  return fmaxf(u2f(t[0]), u2f(t[1]));
}
DI float xrow_sum(float x) {
  auto s = __builtin_amdgcn_permlane16_swap(f2u(x), f2u(x), false, false);
  x = u2f(s[0]) + u2f(s[1]);
  auto t = __builtin_amdgcn_permlane32_swap(f2u(x), f2u(x), false, false);
  return u2f(t[0]) + u2f(t[1]);
}
DI float wave_sum(float x) {
  x += dpp<XOR1>(x); x += dpp<XOR2>(x); x += dpp<HMIR>(x); x += dpp<MIR>(x);
  return xrow_sum(x);
}
DI __amdgpu_buffer_rsrc_t rsrc(const void* p, unsigned bytes) {
  return __builtin_amdgcn_make_buffer_rsrc(const_cast<void*>(p), 0, bytes, 0x00020000);
}
template <int AUX> DI v4u ld128(__amdgpu_buffer_rsrc_t r, unsigned voff, unsigned soff) {
  return __builtin_amdgcn_raw_buffer_load_b128(r, voff, soff, AUX);
}
template <int AUX> DI v2u ld64(__amdgpu_buffer_rsrc_t r, unsigned voff, unsigned soff) {
  return __builtin_amdgcn_raw_buffer_load_b64(r, voff, soff, AUX);
}
DI void st128(__amdgpu_buffer_rsrc_t r, unsigned voff, unsigned soff, v4u v) { __builtin_amdgcn_raw_buffer_store_b128(v, r, voff, soff, SC1); }
DI void st64(__amdgpu_buffer_rsrc_t r, unsigned voff, unsigned soff, v2u v) { __builtin_amdgcn_raw_buffer_store_b64(v, r, voff, soff, SC1); }
DI v2u trread(LAS unsigned char* p) {
  return __builtin_bit_cast(v2u, __builtin_amdgcn_ds_read_tr16_b64_v4i16((LAS s16x4*)p));
}
DI bool tags_ok(v4u g, unsigned T) { return (g[1] == T) & (g[3] == T); }
DI void wg_barrier() {
  asm volatile("s_waitcnt lgkmcnt(0)" ::: "memory");
  __builtin_amdgcn_s_barrier();
  asm volatile("" ::: "memory");
}
template <int N> DI void wait_vm() { asm volatile("s_waitcnt vmcnt(%0)" ::"i"(N) : "memory"); }
DI unsigned clock_now() { return (unsigned)__builtin_amdgcn_s_memrealtime(); }
DI void hold(unsigned t) {
  asm volatile(
      "1:\n\ts_memrealtime vcc\n\ts_waitcnt lgkmcnt(0)\n\ts_sub_u32 vcc_lo, vcc_lo, %0\n\ts_cmp_lt_i32 vcc_lo, 0\n\t"
      "s_cbranch_scc0 2f\n\ts_sleep 1\n\ts_branch 1b\n2:"
      : : "s"(t) : "vcc", "scc", "memory");
}

DI v4u wfrag(LAS unsigned char* tile, int lane, int ks, int cq) {
  const int g = lane >> 4, q4 = (lane & 15) >> 2, p = lane & 3;
  LAS unsigned char* ta = tile + (32 * ks + 8 * g + q4) * TP_STR + 32 * cq + 8 * p;
  v2u a0 = trread(ta), a1 = trread(ta + 4 * TP_STR);
  return v4u{a0[0], a0[1], a1[0], a1[1]};
}
DI void slice_product(LAS unsigned char* stg, int lane, v4u b0, v4u b1) {
  f32x4 c[4];
#pragma unroll
  for (int cq = 0; cq < 4; ++cq) {
    c[cq] = mfma32(wfrag(stg, lane, 0, cq), b0, f32x4{0, 0, 0, 0});
    c[cq] = mfma32(wfrag(stg, lane, 1, cq), b1, c[cq]);
  }
#pragma unroll
  for (int cq = 0; cq < 4; ++cq) *(LAS f32x4*)(stg + cq * 1024 + lane * 16) = c[cq];
  asm volatile("s_waitcnt lgkmcnt(0)" ::: "memory");
}
template <typename V> DI V slices_sum(LAS unsigned char* lds, int off) {
  V c[8];
#pragma unroll
  for (int u = 0; u < 8; ++u) c[u] = *(LAS V*)(lds + L_STG + u * STG + off);
  return ((c[0] + c[1]) + (c[2] + c[3])) + ((c[4] + c[5]) + (c[6] + c[7]));
}
DI unsigned arrive(LAS unsigned char* cnt, int lane) {
  unsigned arr = 0;
  if (lane == 0) arr = __hip_atomic_fetch_add((LAS unsigned*)cnt, 1u, __ATOMIC_RELAXED, __HIP_MEMORY_SCOPE_WORKGROUP);
  arr = __builtin_amdgcn_readfirstlane(arr);
  asm volatile("" ::: "memory");
  return arr;
}
DI void wait_cnt(LAS unsigned char* cnt, unsigned n) {
  SPIN(CAP_L) {
    const unsigned c = __hip_atomic_load((LAS unsigned*)cnt, __ATOMIC_RELAXED, __HIP_MEMORY_SCOPE_WORKGROUP);
    asm volatile("" ::: "memory");
    if (__builtin_amdgcn_readfirstlane(c) == n) break;
    __builtin_amdgcn_s_sleep(1);
  }
}
template <int I> struct IC { static constexpr int v = I; };
template <int A, int B, typename F> DI void static_for(F&& f) {
  if constexpr (A < B) {
    f(IC<A>{});
    static_for<A + 1, B>(f);
  }
}
constexpr int pv_done(int i, int NS, int NR, int NG) {
  int done = 0;
  for (int k = NR + NG + 1; k <= i; ++k)
    for (int u = 0; u < 3; ++u)
      if (done <= k - NS - WIN) ++done;
  return done;
}

struct Args {
  const uint16_t *kc, *vc, *wq, *w1, *w2, *x, *b1;
  unsigned char* ws;
  const uint16_t *wo, *b2, *gw;
  uint16_t* out;
};
static_assert(sizeof(Args) == 96, "no padding");
struct Role {
  int grp, sp, tile;
  bool isQ, isH, isF;
};
struct Early {
  v4u pf, f4[4], lt[4], bf[2];
  v2u lb1[2];
  unsigned tch, E;
};

template <bool HEAVY>
DI void body(const Args& A, const Role& R, LAS unsigned char* lds, const Early& Y, unsigned long long t0) {
  constexpr int NS = HEAVY ? NSH : NSL, NR = HEAVY ? NRH : NRL, NG = (NS + 3) / 4, QPOS = NR - WIN - 2;
  constexpr int T4 = HEAVY ? TH0 : TL0, G1 = HEAVY ? G1H : G1L;
  const int j = blockIdx.x, tid = threadIdx.x, lane = tid & 63;
  const int w = __builtin_amdgcn_readfirstlane(tid >> 6);
  const int x8 = j & 7, r32 = j >> 3;
  const int sp = R.sp, b = R.grp >> 3, kvh = R.grp & 7;
  const bool isQ = HEAVY && R.isQ, isH = HEAVY && R.isH, isF = HEAVY && R.isF;
  const int fks = (R.tile - 28) >> 3, fns = (R.tile - 28) & 7;
  const int hd = x8 * 4 + (r32 >> 3), ns = (r32 + 6) & 7;
  const int fb = r32 >> 2, fq = r32 & 3;
  const __amdgpu_buffer_rsrc_t rws = rsrc(A.ws, WS_END);
  LAS unsigned char* cnt = lds + L_CNT;

  const __amdgpu_buffer_rsrc_t rk = rsrc(A.kc + (size_t)R.grp * 4096 * 128, 1 << 20), rv = rsrc(A.vc + (size_t)R.grp * 4096 * 128, 1 << 20);
  const unsigned qsel = ((lane & 15) >> 2) * 256 + (lane >> 4) * 64 + (lane & 3) * 16;
  const unsigned sbase = (((sp >> 1) * 64 + (HEAVY ? 0 : NSH)) * 8 + w) * 1024;
  const unsigned koff = qsel + sbase, voff = lane * 16 + sbase;
  v4u kv[NR];
  unsigned tb = (unsigned)t0, tf = 0;
  auto sched = [](int i) constexpr { return T4 + (TEND - T4) * (i - 4) / (2 * NS - 5); };
  auto slot_load = [&](int i) INL {
    if (i >= 4 && i % 2 == 0) {
      if ((i > G1 + WIN && i <= G1 + WIN + 6) || (i >= NR && i < NR + 2)) tb = __builtin_amdgcn_readfirstlane(*(LAS unsigned*)(cnt + C_ORG));
      hold(tb + sched(i));
    }
    kv[i % NR] = i < NS ? ld128<NT>(rk, koff, i * 8192) : ld128<NT>(rv, voff, (i - NS) * 8192);
  };
  LAS unsigned char* stg = lds + L_STG + w * STG;
  unsigned E = Y.E, T;
  auto tag = [&]() INL {
    asm volatile("s_waitcnt lgkmcnt(0)" : "+s"(E) : : "memory");
    T = E * 1664525u + 1013904223u;
  };
  if (tid < 10) *(LAS unsigned*)(cnt + tid * 4) = tid == C_ORG / 4 ? tb : 0;
  wg_barrier();
  if (!HEAVY) {
#pragma unroll
    for (int i = 0; i < 4; ++i) kv[i] = Y.f4[i];
    wait_vm<4>();
    tf = clock_now();
  } else {
#pragma unroll
    for (int i = 0; i < 4; ++i) {
      wait_vm<8>();
      slot_load(i);
      SCHED_FENCE();
    }
    tag();
#pragma unroll
    for (int i = 0; i < 8; ++i) *(LAS v4u*)(stg + (8 * i + (lane >> 3)) * TP_STR + (lane & 7) * 16) = i < 4 ? Y.f4[i] : Y.lt[i - 4];
    if (isQ | isH) {
      slice_product(stg, lane, Y.bf[0], Y.bf[1]);
      if (arrive(cnt + C_TILE, lane) == 0) {
        wait_cnt(cnt + C_TILE, 8);
#pragma unroll
        for (int u = 0; u < 2; ++u) {
          const int cq = 2 * u + ((lane >> 3) & 1);
          f32x4 c = slices_sum<f32x4>(lds, cq * 1024 + (lane & 0x37) * 16);
          if (isH) {
            c[0] = rbf(rbf(c[0]) + bf_lo(Y.lb1[u][0])); c[1] = rbf(rbf(c[1]) + bf_hi(Y.lb1[u][0]));
            c[2] = rbf(rbf(c[2]) + bf_lo(Y.lb1[u][1])); c[3] = rbf(rbf(c[3]) + bf_hi(Y.lb1[u][1]));
#pragma unroll
            for (int r = 0; r < 4; ++r) { c[r] = c[r] > 0.f ? c[r] : 0.f; c[r] = c[r] * c[r]; }
          }
          st128(rws, isQ ? (lane & 7) * 16384 + cq * 64 + (lane >> 4) * 16 : cq * 512 + (lane >> 4) * 128 + (lane & 7) * 16,
                isQ ? WS_QG + R.tile * 256 : WS_HG + R.tile * 2048, v4u{pk_bf16(c[0], c[1]), T, pk_bf16(c[2], c[3]), T});
        }
      }
    }
    asm volatile("" ::"v"(Y.pf), "v"(Y.tch));
  }
  SCHED_FENCE();

  float gate = 0.f;
  v4u qg[2], hg[2], lgx, lgw, tg;
  unsigned lb2;
  bool lead;
  auto rep_load = [&]() INL { tg = ld128<SC1>(rws, lead ? lane * 16 : OOB, WS_T0); };
  auto rep_mean = [&](int late) INL {
    float e[2];
#pragma unroll
    for (int u = 0; u < 2; ++u) e[u] = (float)(tg[2 * u + 1] == T ? min(max((int)(tg[2 * u] - (unsigned)t0), -1000), 1000) : late);
    float d = wave_sum(e[0] + e[1]) * (1.f / 128.f);
    d += wave_sum(fabsf(e[0] - d) + fabsf(e[1] - d)) * (0.5f / 128.f);
    *(LAS unsigned*)(cnt + C_ORG) = (unsigned)t0 + (int)d + OB;
  };
  const bool hpad = lane >= 48;
  auto qh_load = [&]() INL {
#pragma unroll
    for (int u = 0; u < 2; ++u) qg[u] = ld128<SC1>(rws, lead ? lane * 16 + u * 1024 : OOB, WS_QG + (b * 2048 + kvh * 256) * 8);
    if (HEAVY) {
      const unsigned so = isF ? WS_HG + (8 * fks + w) * 1792 : 0;
      hg[0] = ld128<SC1>(rws, isF ? lane * 16 : OOB, so);
      hg[1] = ld128<SC1>(rws, (isF && !hpad) ? lane * 16 + 1024 : OOB, so);
    }
  };
  auto qh_ok = [&]() INL {
    bool ok = !lead | (tags_ok(qg[0], T) & tags_ok(qg[1], T));
    if (HEAVY) ok &= !isF | (tags_ok(hg[0], T) & (hpad | tags_ok(hg[1], T)));
    return __all(ok);
  };
  static_assert(NS <= NR && G1 + WIN <= GPOS && (HEAVY || 5 + WIN <= G1) && GPOS + WIN <= QPOS, "");
  static_for<4, NR>([&](auto ic) INL {
    constexpr int i = decltype(ic)::v;
    if (i == 5 && !HEAVY) {
      tag();
      const bool me = arrive(cnt + C_REP, lane) == 0;
      st64(rws, (me && lane == 0) ? 0 : OOB, WS_T0 + (j >> 1) * 8, v2u{(int)(tf - AL0 - tb) > 0 ? tf - AL0 : tb, T});
      SCHED_FENCE();
    }
    if (i == WIN + 2 && !HEAVY) asm volatile("" ::"v"(Y.pf), "v"(Y.tch));
    if (i == G1) {
      lead = arrive(cnt + C_RLEAD, lane) == 0;
      rep_load();
      SCHED_FENCE();
    }
    if (i == G1 + WIN) {
      if (lead) rep_mean(sched(G1) - 250);
      SCHED_FENCE();
    }
    if (i == GPOS) {
      lgx = ld128<0>(rsrc(A.x, 8192), w == 4 ? lane * 16 : OOB, fb * 1024);
      lgw = ld128<0>(rsrc(A.gw, 1024), w == 4 ? lane * 16 : OOB, 0);
      lb2 = __builtin_amdgcn_raw_buffer_load_b32(rsrc(A.b2, 1024), w == 4 ? (lane & 7) * 4 : OOB, (64 * x8 + 16 * fq) * 2, 0);
      SCHED_FENCE();
    }
    if (i == GPOS + WIN) {
      if (w == 4) {
        float t = 0.f;
#pragma unroll
        for (int u = 0; u < 4; ++u) t += rbf(bf_lo(lgx[u]) * bf_lo(lgw[u])) + rbf(bf_hi(lgx[u]) * bf_hi(lgw[u]));
        t = rbf(wave_sum(t));
        gate = rbf(1.f / (1.f + __expf(-t)));
      }
      asm volatile("" : "+v"(gate));
      SCHED_FENCE();
    }
    if (i == QPOS) {
      lead = arrive(cnt + C_QLEAD, lane) == 0;
      qh_load();
      rep_load();
      SCHED_FENCE();
    }
    if (i == QPOS + WIN) {
      if (!qh_ok()) {
        SPIN(CAP_G) {
          __builtin_amdgcn_s_sleep(2);
          qh_load();
          const bool ok = qh_ok();
          asm volatile("" ::: "memory");
          if (ok) break;
        }
      }
      if (lead) {
#pragma unroll
        for (int u = 0; u < 2; ++u) *(LAS v2u*)(lds + L_QS + u * 512 + lane * 8) = v2u{qg[u][0], qg[u][2]};
        rep_mean(sched(QPOS) - 250);
        asm volatile("s_waitcnt lgkmcnt(0)" ::: "memory");
        __hip_atomic_store((LAS unsigned*)(cnt + C_QRDY), 1u, __ATOMIC_RELAXED, __HIP_MEMORY_SCOPE_WORKGROUP);
      }
      if (HEAVY && isF) {
        v4u hf[2];
        const bool hi = lane & 8;
#pragma unroll
        for (int s = 0; s < 2; ++s) {
          const unsigned o0 = dppu<ROR8>(hg[s][0]), o1 = dppu<ROR8>(hg[s][2]);
          hf[s] = hi ? v4u{o0, o1, hg[s][0], hg[s][2]} : v4u{hg[s][0], hg[s][2], o0, o1};
        }
        slice_product(stg, lane, hf[0], hf[1]);
        arrive(cnt + C_FFN, lane);
      }
      SCHED_FENCE();
    }
    if (i >= QPOS && i < QPOS + WIN) wait_vm<WIN - 1 + (HEAVY ? 5 : 3)>();
    else if (i >= GPOS && i < GPOS + WIN) wait_vm<WIN - 1 + 3>();
    else if ((!HEAVY && i >= 5 && i < 5 + WIN) || (i >= G1 && i < G1 + WIN)) wait_vm<WIN>();
    else wait_vm<WIN - 1>();
    slot_load(i);
    SCHED_FENCE();
  });
  wait_cnt(cnt + C_QRDY, 1);
  const v4u qf = *(LAS v4u*)(lds + L_QS + qsel);
  SCHED_FENCE();

  const int cc = lane & 3;
  const unsigned mask0 = cc == 0 ? 0x0000ffffu : cc == 1 ? 0xffff0000u : 0u;
  const unsigned mask1 = cc == 2 ? 0x0000ffffu : cc == 3 ? 0xffff0000u : 0u;
  constexpr float C2 = 0.08838834764831845f * 1.4426950408889634f;
  float S[NG];
  unsigned pp[NG];
  f32x4 a0 = {0, 0, 0, 0}, a1 = {0, 0, 0, 0};
  auto score = [&](v4u k) INL {
    const f32x4 d = mfma32(k, qf, f32x4{0, 0, 0, 0});
    return (dpp<0x00>(d[0]) + dpp<0x55>(d[1])) + (dpp<0xAA>(d[2]) + dpp<0xFF>(d[3]));
  };
  auto pv = [&](int i) INL {
    const unsigned pb = (i & 3) == 0 ? dppu<0x00>(pp[i >> 2]) : (i & 3) == 1 ? dppu<0x55>(pp[i >> 2]) : (i & 3) == 2 ? dppu<0xAA>(pp[i >> 2]) : dppu<0xFF>(pp[i >> 2]);
    const v2u pa = {pb & mask0, pb & mask1};
    const v4u v = kv[(NS + i) % NR];
    a0 = mfma16(pa, v2u{v[0], v[1]}, a0);
    a1 = mfma16(pa, v2u{v[2], v[3]}, a1);
  };
  const unsigned sf = WS_FG + (x8 * 32 + r32) * 512;
  v4u fg, lo[2];
  float f0 = 0.f, f1 = 0.f;
  bool fok = false;
  auto ffn_part = [&]() INL {
    f0 = lane < 32 ? u2f(fg[0]) : 0.f;
    f1 = lane < 32 ? u2f(fg[2]) : 0.f;
    f0 += dpp<ROR8>(f0); f1 += dpp<ROR8>(f1);
    f0 = xrow_sum(f0); f1 = xrow_sum(f1);
    const float og = rbf(1.f - gate);
    f0 = rbf(og * rbf(rbf(f0) + bf_lo(lb2)));
    f1 = rbf(og * rbf(rbf(f1) + bf_hi(lb2)));
    asm volatile("" : "+v"(f0), "+v"(f1));
  };
  constexpr int FS = NR + 3, FP = 2 * NS - 10 - WIN;
  static_assert(2 * NS - NR >= NG + 2 + WIN && HP <= WIN && FS + WIN + 4 <= FP, "");
  static_for<NR, 2 * NS>([&](auto ic) INL {
    constexpr int i = decltype(ic)::v, t = i - NR;
    if constexpr (t < NG) {
      const float s0 = score(kv[4 * t]), s1 = 4 * t + 1 < NS ? score(kv[(4 * t + 1) % NR]) : -INFINITY;
      const float s2 = 4 * t + 2 < NS ? score(kv[(4 * t + 2) % NR]) : -INFINITY, s3 = 4 * t + 3 < NS ? score(kv[(4 * t + 3) % NR]) : -INFINITY;
      S[t] = (cc & 2) ? ((cc & 1) ? s3 : s2) : ((cc & 1) ? s1 : s0);
    }
    if constexpr (t == NG) {
      float m = S[0];
#pragma unroll
      for (int u = 1; u < NG; ++u) m = fmaxf(m, S[u]);
      m = fmaxf(m, dpp<XOR1>(m));
      m = fmaxf(m, dpp<XOR2>(m));
      m = xrow_max(m) * C2;
      float l = 0.f;
#pragma unroll
      for (int u = 0; u < NG; ++u) {
        const float p = __builtin_amdgcn_exp2f(__builtin_fmaf(S[u], C2, -m));
        l += p;
        pp[u] = pk_bf16(p, p);
      }
      l += dpp<XOR1>(l);
      l += dpp<XOR2>(l);
      l = xrow_sum(l);
      if ((lane & 0x33) == 0) {
        *(LAS float*)(lds + L_ML + (lane >> 2) * 32 + w * 4) = m;
        *(LAS float*)(lds + L_ML + 128 + (lane >> 2) * 32 + w * 4) = l;
      }
      asm volatile("s_waitcnt lgkmcnt(0)" ::: "memory");
      if (arrive(cnt + C_SOFT, lane) == 7) {
        const float mw = *(LAS float*)(lds + L_ML + (lane & 31) * 4), lw = *(LAS float*)(lds + L_ML + 128 + (lane & 31) * 4);
        float M = fmaxf(mw, dpp<XOR1>(mw));
        M = fmaxf(M, dpp<XOR2>(M));
        M = fmaxf(M, dpp<HMIR>(M));
        const float ex = __builtin_amdgcn_exp2f(mw - M);
        float L = ex * lw;
        L += dpp<XOR1>(L);
        L += dpp<XOR2>(L);
        L += dpp<HMIR>(L);
        *(LAS float*)(lds + L_ML + 256 + (lane & 31) * 4) = ex * __builtin_amdgcn_rcpf(L);
        *(LAS float*)(lds + L_ML + 384 + ((lane >> 3) & 3) * 4) = M + __builtin_amdgcn_logf(L);
        asm volatile("s_waitcnt lgkmcnt(0)" ::: "memory");
      }
    }
    if constexpr (HEAVY && i == FS) {
      f32x2 c = {0, 0};
      if (isF && w < 4) {
        wait_cnt(cnt + C_FFN, 8);
        c = slices_sum<f32x2>(lds, w * 1024 + (lane & 0x37) * 16 + (lane & 8));
      }
      st128(rws, (isF && w < 4) ? (lane & 7) * 2048 + (lane >> 4) * 32 + (lane & 8) * 2 : OOB,
            isF ? WS_FG + ((fns * 32 + (w & 3)) * 64 + fks * 16) * 8 : 0, v4u{f2u(c[0]), T, f2u(c[1]), T});
    }
    if constexpr (i == FP) fg = ld128<SC1>(rws, w == 4 ? (lane & 31) * 16 : OOB, sf);
    if constexpr (i == FP + WIN) {
      if (w == 4) {
        fok = __all(tags_ok(fg, T));
        ffn_part();
      }
    }
    SCHED_FENCE();
    if constexpr ((i >= FP && i < FP + WIN) || (HEAVY && i >= FS && i < FS + WIN)) wait_vm<WIN>();
    else wait_vm<WIN - 1>();
    slot_load(i);
    SCHED_FENCE();
    static_for<pv_done(i - 1, NS, NR, NG), pv_done(i, NS, NR, NG)>([&](auto uc) INL { pv(decltype(uc)::v); });
    SCHED_FENCE();
  });
  static_for<pv_done(2 * NS - 1, NS, NR, NG), NS - 2>([&](auto uc) INL {
    constexpr int u = decltype(uc)::v;
    if constexpr (u == NS - HP) {
      const bool last = arrive(cnt + C_ANN1, lane) == 7;
      const unsigned lse = *(LAS unsigned*)(lds + L_ML + 384 + (lane & 3) * 4);
      st64(rws, (last && lane < 4) ? lane * 8 : OOB, WS_LG + ((kvh * 8 + b) * 4 + sp) * 128, v2u{lse, T});
      SCHED_FENCE();
    }
    pv(u);
    SCHED_FENCE();
  });
  wait_vm<1>();
#pragma unroll
  for (int u = 0; u < 2; ++u)
    lo[u] = ld128<NT>(rsrc(A.wo, 4096 * 1024), (lane >> 3) * 1024 + (lane & 7) * 16, (128 * hd + 16 * w + 8 * u) * 1024 + ns * 128);
  SCHED_FENCE();
  pv(NS - 2);
  pv(NS - 1);
  *(LAS f32x4*)(lds + L_MRG + w * 2048 + lane * 32) = a0;
  *(LAS f32x4*)(lds + L_MRG + w * 2048 + lane * 32 + 16) = a1;
  wg_barrier();

  if (w < 4) {
    const f32x4 g0 = *(LAS f32x4*)(lds + L_ML + 256 + w * 32), g1 = *(LAS f32x4*)(lds + L_ML + 256 + w * 32 + 16);
    f32x2 ov[8];
#pragma unroll
    for (int u = 0; u < 8; ++u) ov[u] = *(LAS f32x2*)(lds + L_MRG + u * 2048 + w * 512 + lane * 8);
    const f32x2 o = ((ov[0] * g0[0] + ov[1] * g0[1]) + (ov[2] * g0[2] + ov[3] * g0[3])) + ((ov[4] * g1[0] + ov[5] * g1[1]) + (ov[6] * g1[2] + ov[7] * g1[3]));
    const unsigned pk = pk_bf16(o[0], o[1]), nb = dppu<XOR1>(pk);
    st128(rws, (lane & 1) ? OOB : lane * 8, WS_OG + (((4 * kvh + w) * 8 + b) * 4 + sp) * 512, v4u{pk, T, nb, T});
  }
  SCHED_FENCE();
  asm volatile("" ::: "memory");
  __builtin_amdgcn_s_barrier();
  asm volatile("" ::: "memory");

  v4u wf[4];
  {
    const unsigned so = WS_OG + (hd * 8 + w) * 2048, sl = WS_LG + ((hd >> 2) * 8 + w) * 512 + (hd & 3) * 8;
    v4u og[2];
    v2u lg;
    SPIN(CAP_G) {
      lg = ld64<SC1>(rws, cc * 128, sl);
      const bool ok = __all(lg[1] == T);
      asm volatile("" ::: "memory");
      if (ok) break;
    }
#pragma unroll
    for (int s = 0; s < 2; ++s) og[s] = ld128<SC1>(rws, lane * 16 + s * 1024, so);
    SCHED_FENCE();
    {
      const unsigned now = clock_now();
      st64(rws, (arrive(cnt + C_ANN2, lane) == 7 && lane == 0) ? 0 : OOB, WS_H2 + (ns * 32 + hd) * 128, v2u{now, T});
    }
    const float ls = u2f(lg[0]);
    float M = fmaxf(ls, dpp<XOR1>(ls));
    M = fmaxf(M, dpp<XOR2>(M));
    const float ex = __builtin_amdgcn_exp2f(ls - M);
    float Z = ex + dpp<XOR1>(ex);
    Z += dpp<XOR2>(Z);
    const float wn = ex * __builtin_amdgcn_rcpf(Z);
    const float m0 = dpp<0x00>(wn), m1 = dpp<0x55>(wn), m2 = dpp<0xAA>(wn), m3 = dpp<0xFF>(wn);
    float w0 = lane < 32 ? m0 : m1, w1 = lane < 32 ? m2 : m3;
    asm volatile("" : "+v"(w0), "+v"(w1));
#pragma unroll
    for (int u = 0; u < 2; ++u) *(LAS v4u*)(lds + L_TO + (16 * w + 8 * u + (lane >> 3)) * TP_STR + (lane & 7) * 16) = lo[u];
    wg_barrier();
#pragma unroll
    for (int s = 0; s < 4; ++s) wf[s] = wfrag(lds + L_TO, lane, s, w & 3);
    SCHED_FENCE();
    if (!__all(tags_ok(og[0], T) & tags_ok(og[1], T))) {
      SPIN(CAP_G) {
#pragma unroll
        for (int s = 0; s < 2; ++s) og[s] = ld128<SC1>(rws, lane * 16 + s * 1024, so);
        const bool ok = __all(tags_ok(og[0], T) & tags_ok(og[1], T));
        asm volatile("" ::: "memory");
        if (ok) break;
      }
    }
    const float o0 = w0 * bf_lo(og[0][0]) + w1 * bf_lo(og[1][0]), o1 = w0 * bf_hi(og[0][0]) + w1 * bf_hi(og[1][0]);
    const float o2 = w0 * bf_lo(og[0][2]) + w1 * bf_lo(og[1][2]), o3 = w0 * bf_hi(og[0][2]) + w1 * bf_hi(og[1][2]);
    auto x0 = __builtin_amdgcn_permlane32_swap(f2u(o0), f2u(o2), false, false);
    auto x1 = __builtin_amdgcn_permlane32_swap(f2u(o1), f2u(o3), false, false);
    *(LAS unsigned*)(lds + L_OS + w * OS_STR + (lane & 31) * 8 + (lane >> 5) * 4) = pk_bf16(u2f(x0[0]) + u2f(x0[1]), u2f(x1[0]) + u2f(x1[1]));
  }
  wg_barrier();

  if (w < 4) {
    v4u ob[4];
#pragma unroll
    for (int s = 0; s < 4; ++s) ob[s] = *(LAS v4u*)(lds + L_OS + (lane & 7) * OS_STR + s * 64 + (lane >> 4) * 16);
    f32x4 c = mfma32(wf[0], ob[0], f32x4{0, 0, 0, 0}), c2 = mfma32(wf[1], ob[1], f32x4{0, 0, 0, 0});
    c = mfma32(wf[2], ob[2], c);
    c2 = mfma32(wf[3], ob[3], c2);
    c += c2;
    const bool hi = lane & 8;
    st128(rws, (lane & 7) * 16384 + (lane >> 4) * 32 + (lane & 8) * 2, WS_PG + ((ns * 32 + w) * 512 + hd * 16) * 8,
          v4u{f2u(hi ? c[2] : c[0]), T, f2u(hi ? c[3] : c[1]), T});
  } else if (w == 4) {
    const unsigned sp_ = WS_PG + (x8 * 32 + r32) * 4096;
    if (!fok) {
      SPIN(CAP_G) {
        fg = ld128<SC1>(rws, (lane & 31) * 16, sf);
        const bool ok = __all(tags_ok(fg, T));
        asm volatile("" ::: "memory");
        if (ok) break;
      }
      ffn_part();
    }
    v2u hn;
    SPIN(CAP_G) {
      hn = ld64<SC1>(rws, (lane & 31) * 128, WS_H2 + x8 * 4096);
      asm volatile("" ::: "memory");
      if (__all(hn[1] == T)) break;
    }
    {
      int d = (int)(hn[0] - tb);
      d = max(d, (int)dppu<XOR1>(d));
      d = max(d, (int)dppu<XOR2>(d));
      d = max(d, (int)dppu<HMIR>(d));
      d = max(d, (int)dppu<MIR>(d));
      hold(tb + min(max(__builtin_amdgcn_readlane(d, 0), __builtin_amdgcn_readlane(d, 16)), 10000) + C2L);
    }
    v4u pg[4];
    SPIN(CAP_G) {
#pragma unroll
      for (int i = 0; i < 4; ++i) pg[i] = ld128<SC1>(rws, lane * 16 + i * 1024, sp_);
      const bool ok = __all(tags_ok(pg[0], T) & tags_ok(pg[1], T) & tags_ok(pg[2], T) & tags_ok(pg[3], T));
      asm volatile("" ::: "memory");
      if (ok) break;
    }
    float s0 = (u2f(pg[0][0]) + u2f(pg[1][0])) + (u2f(pg[2][0]) + u2f(pg[3][0]));
    float s1 = (u2f(pg[0][2]) + u2f(pg[1][2])) + (u2f(pg[2][2]) + u2f(pg[3][2]));
    s0 += dpp<ROR8>(s0); s1 += dpp<ROR8>(s1);
    s0 = xrow_sum(s0); s1 = xrow_sum(s1);
    const float r0 = rbf(gate * rbf(s0)) + f0;
    const float r1 = rbf(gate * rbf(s1)) + f1;
    __builtin_amdgcn_raw_buffer_store_b32(pk_bf16(r0, r1), rsrc(A.out, 8192), lane < 8 ? lane * 4 : OOB, (fb * 512 + 64 * x8 + 16 * fq) * 2, SC1);
    __builtin_amdgcn_raw_buffer_store_b32(T, rws, (j == 0 && lane == 0) ? 0 : OOB, 0, SC1);
  }
}

__global__ void __launch_bounds__(512, 2) fused_decode(Args) {
  __shared__ __attribute__((aligned(16))) unsigned char lds_raw[L_END];
  unsigned long long t0;
  u64x8 ka;
  u64x4 kb;
  asm volatile("s_load_dwordx16 %0, %3, 0x0\n\ts_load_dwordx8 %1, %3, 0x40\n\ts_memrealtime %2"
               : "=&s"(ka), "=&s"(kb), "=&s"(t0) : "s"(__builtin_amdgcn_kernarg_segment_ptr()));
  SCHED_FENCE();
  unsigned long long pc;
  asm volatile("s_getpc_b64 %0" : "=s"(pc));
  Early Y;
  Y.pf = ld128<0>(rsrc((const void*)(pc & ~255ull), CODEB), blockIdx.x < 32 ? threadIdx.x * 16 + (blockIdx.x >> 3) * 8192 : OOB, 0);
  SCHED_FENCE();
  const int j = blockIdx.x, lane = threadIdx.x & 63, w = __builtin_amdgcn_readfirstlane(threadIdx.x >> 6);
  const int x8 = j & 7, r32 = j >> 3;
  Role R;
  R.grp = x8 * 8 + (r32 >> 2);
  R.sp = (r32 + x8) & 3;
  R.tile = (r32 >> 2) * 8 + ((x8 + (r32 >> 2)) & 7);
  const bool heavy = (R.sp & 1) == 0;
  R.isQ = R.sp == 0;
  R.isH = R.sp == 2 && R.tile < 28;
  R.isF = R.sp == 2 && R.tile >= 28 && R.tile < 60;
  unsigned oK = (R.grp << 20) + (((R.sp >> 1) * 64 + NSH) * 8 + w) * 1024, oQ = w * (64 * 8192) + R.tile * 128;
  unsigned oH = w * (64 * 3584) + R.tile * 128, oF = (448 * ((R.tile - 28) >> 3) + 56 * w) * 1024 + ((R.tile - 28) & 7) * 128;
  asm volatile("" : "+s"(oK), "+s"(oQ), "+s"(oH), "+s"(oF));
  const bool off = heavy && !(R.isQ | R.isH | R.isF);
  unsigned rs = off ? 0 : R.isQ ? 8192 : R.isH ? 3584 : 1024;
  unsigned bytes = off ? 0 : !heavy ? 1 << 26 : R.isQ ? 512 * 8192 : 1792 * 1024;
  unsigned org = !heavy ? oK : R.isQ ? oQ : R.isH ? oH : oF;
  asm volatile("" : "+s"(rs), "+s"(bytes), "+s"(org));
  unsigned lk = ((lane & 15) >> 2) * 256 + (lane >> 4) * 64 + (lane & 3) * 16, lw = (lane >> 3) * rs + (lane & 7) * 16;
  asm volatile("" : "+v"(lk), "+v"(lw));
  unsigned vo = org + (heavy ? lw : lk);
  asm volatile("" : "+v"(vo));
  SCHED_FENCE();
  asm volatile("s_waitcnt lgkmcnt(0)" : "+s"(ka), "+s"(kb), "+s"(t0));
  Args A;
  A.kc = (const uint16_t*)ka[0]; A.vc = (const uint16_t*)ka[1]; A.wq = (const uint16_t*)ka[2]; A.w1 = (const uint16_t*)ka[3];
  A.w2 = (const uint16_t*)ka[4]; A.x = (const uint16_t*)ka[5]; A.b1 = (const uint16_t*)ka[6]; A.ws = (unsigned char*)ka[7];
  A.wo = (const uint16_t*)kb[0]; A.b2 = (const uint16_t*)kb[1]; A.gw = (const uint16_t*)kb[2]; A.out = (uint16_t*)kb[3];
  asm volatile("s_load_dword %0, %1, 0x0" : "=s"(Y.E) : "s"(A.ws) : "memory");
  const __amdgpu_buffer_rsrc_t r0 = rsrc(!heavy ? A.kc : R.isQ ? A.wq : R.isH ? A.w1 : A.w2, bytes);
#pragma unroll
  for (int i = 0; i < 4; ++i) Y.f4[i] = ld128<NT>(r0, vo, 8 * i * rs);
  SCHED_FENCE();
  if (heavy) {
#pragma unroll
    for (int i = 0; i < 4; ++i) Y.lt[i] = ld128<NT>(r0, (i == 3 && R.isF) ? OOB : vo, 8 * (i + 4) * rs);
#pragma unroll
    for (int s = 0; s < 2; ++s) Y.bf[s] = ld128<0>(rsrc(A.x, 8192), (lane & 7) * 1024 + (lane >> 4) * 16 + s * 64, w * 128);
#pragma unroll
    for (int u = 0; u < 2; ++u) Y.lb1[u] = ld64<0>(rsrc(A.b1, 1792 * 2), R.isH ? ((lane >> 3) & 1) * 32 + (lane >> 4) * 8 + u * 64 : OOB, R.isH ? R.tile * 128 : 0);
  }
  SCHED_FENCE();
  {
    unsigned long long c0 = ka[1] + oK, c1 = ka[0] + oK, c2 = kb[0] + (x8 * 4 + (r32 >> 3)) * 131072, c3 = ka[7] + 1024;
    asm volatile("" : "+s"(c0), "+s"(c1), "+s"(c2), "+s"(c3));
    unsigned long long d0 = (w & 1) ? c1 : c0, d1 = (w & 1) ? c3 : c2, d2 = (w & 1) ? kb[3] : ka[5], d3 = (w & 1) ? kb[1] : kb[2];
    asm volatile("" : "+s"(d0), "+s"(d1), "+s"(d2), "+s"(d3));
    d0 = (w & 2) ? d1 : d0;
    d2 = (w & 2) ? d3 : d2;
    asm volatile("" : "+s"(d0), "+s"(d2));
    Y.tch = __builtin_amdgcn_raw_buffer_load_b32(rsrc((const void*)((w & 4) ? d2 : d0), 4), lane * 4, 0, NT);
  }
  SCHED_FENCE();
  if (__builtin_expect(heavy, 0)) body<true>(A, R, (LAS unsigned char*)lds_raw, Y, t0);
  else body<false>(A, R, (LAS unsigned char*)lds_raw, Y, t0);
}

extern "C" void kernel_launch(void* const* d_in, const int* in_sizes, int n_in, void* d_out, int out_size, void* d_ws, size_t ws_size,
                              hipStream_t stream) {
  Args A;
  A.x = (const uint16_t*)d_in[0]; A.kc = (const uint16_t*)d_in[1]; A.vc = (const uint16_t*)d_in[2];
  A.wq = (const uint16_t*)d_in[3]; A.wo = (const uint16_t*)d_in[4]; A.w1 = (const uint16_t*)d_in[6];
  A.b1 = (const uint16_t*)d_in[7]; A.w2 = (const uint16_t*)d_in[8]; A.b2 = (const uint16_t*)d_in[9];
  A.gw = (const uint16_t*)d_in[10]; A.out = (uint16_t*)d_out; A.ws = (unsigned char*)d_ws;
  fused_decode<<<256, 512, 0, stream>>>(A);
}
```

```cpp
#include <hip/hip_runtime.h>
#include <hip/hip_bf16.h>
#include <stdint.h>

constexpr int WIN = 5;
constexpr int NSH = 30, NSL = 64 - NSH;
constexpr int NRH = 30, NRL = 36;
constexpr int G1H = 4, G1L = 12;
constexpr int GPOS = 18;
constexpr int HP = 3;
constexpr int TEND = 1872;
constexpr int TH0 = 320, TL0 = 130;
constexpr int AL0 = 105;
constexpr int OB = 30;
constexpr int C2L = 109;
constexpr int CODEB = 28672;

typedef unsigned v4u __attribute__((ext_vector_type(4)));
typedef unsigned v2u __attribute__((ext_vector_type(2)));
typedef float f32x4 __attribute__((ext_vector_type(4)));
typedef float f32x2 __attribute__((ext_vector_type(2)));
typedef short s16x4 __attribute__((ext_vector_type(4)));
typedef __bf16 bf16x8 __attribute__((ext_vector_type(8)));
typedef __bf16 bf16x2 __attribute__((ext_vector_type(2)));
typedef unsigned long long u64x8 __attribute__((ext_vector_type(8)));
typedef unsigned long long u64x4 __attribute__((ext_vector_type(4)));
#define LAS __attribute__((address_space(3)))
#define DI __device__ __forceinline__
#define INL __attribute__((always_inline))
#define SCHED_FENCE() __builtin_amdgcn_sched_barrier(0)
#define SPIN(cap) _Pragma("nounroll") for (unsigned spin = 0; spin < (cap); ++spin)

constexpr int NT = 2, SC1 = 16;
constexpr unsigned OOB = 0x7fffff00u;
constexpr unsigned CAP_G = 1u << 21, CAP_L = 1u << 25;
constexpr unsigned WS_T0 = 4096;
constexpr unsigned WS_QG = 8192;
constexpr unsigned WS_HG = WS_QG + 8 * 2048 * 8;
constexpr unsigned WS_OG = WS_HG + 65536;
constexpr unsigned WS_LG = WS_OG + 32 * 32 * 64 * 8;
constexpr unsigned WS_PG = WS_LG + 256 * 128;
constexpr unsigned WS_FG = WS_PG + 256 * 32 * 16 * 8;
constexpr unsigned WS_H2 = WS_FG + 256 * 4 * 16 * 8;
constexpr unsigned WS_END = WS_H2 + 256 * 128;
constexpr int TP_STR = 160, OS_STR = 272, STG = 64 * TP_STR;
constexpr int L_STG = 0;
constexpr int L_TO = 8 * STG;
constexpr int L_MRG = L_TO + 128 * TP_STR;
constexpr int L_OS = L_MRG + 8 * 2048;
constexpr int L_QS = L_OS + 8 * OS_STR;
constexpr int L_ML = L_QS + 1024;
constexpr int L_CNT = L_ML + 400;
constexpr int L_END = L_CNT + 64;
enum { C_TILE = 0, C_ANN1 = 4, C_ANN2 = 8, C_QRDY = 12, C_FFN = 16, C_QLEAD = 20, C_SOFT = 24, C_REP = 28, C_RLEAD = 32, C_ORG = 36 };

DI f32x4 mfma32(v4u a, v4u b, f32x4 c) {
  return __builtin_amdgcn_mfma_f32_16x16x32_bf16(__builtin_bit_cast(bf16x8, a), __builtin_bit_cast(bf16x8, b), c, 0, 0, 0);
}
DI f32x4 mfma16(v2u a, v2u b, f32x4 c) {
  return __builtin_amdgcn_mfma_f32_16x16x16bf16_1k(__builtin_bit_cast(s16x4, a), __builtin_bit_cast(s16x4, b), c, 0, 0, 0);
}
template <int CTRL> DI unsigned dppu(unsigned x) { return (unsigned)__builtin_amdgcn_mov_dpp((int)x, CTRL, 0xf, 0xf, true); }
template <int CTRL> DI float dpp(float x) { return __builtin_bit_cast(float, dppu<CTRL>(__builtin_bit_cast(unsigned, x))); }
constexpr int XOR1 = 0xB1, XOR2 = 0x4E, HMIR = 0x141, MIR = 0x140, ROR8 = 0x128;
DI unsigned pk_bf16(float lo, float hi) {
  f32x2 v = {lo, hi};
  return __builtin_bit_cast(unsigned, __builtin_convertvector(v, bf16x2));
}
DI float rbf(float x) { return __builtin_bit_cast(float, pk_bf16(x, x) & 0xffff0000u); }
DI float bf_lo(unsigned u) { return __builtin_bit_cast(float, u << 16); }
DI float bf_hi(unsigned u) { return __builtin_bit_cast(float, u & 0xffff0000u); }
DI float u2f(unsigned u) { return __builtin_bit_cast(float, u); }
DI unsigned f2u(float f) { return __builtin_bit_cast(unsigned, f); }
DI float xrow_max(float x) {
  auto s = __builtin_amdgcn_permlane16_swap(f2u(x), f2u(x), false, false);
  x = fmaxf(u2f(s[0]), u2f(s[1]));
  auto t = __builtin_amdgcn_permlane32_swap(f2u(x), f2u(x), false, false);
  return fmaxf(u2f(t[0]), u2f(t[1]));
}
DI float xrow_sum(float x) {
  auto s = __builtin_amdgcn_permlane16_swap(f2u(x), f2u(x), false, false);
  x = u2f(s[0]) + u2f(s[1]);
  auto t = __builtin_amdgcn_permlane32_swap(f2u(x), f2u(x), false, false);
  return u2f(t[0]) + u2f(t[1]);
}
DI float wave_sum(float x) {
  x += dpp<XOR1>(x); x += dpp<XOR2>(x); x += dpp<HMIR>(x); x += dpp<MIR>(x);
  return xrow_sum(x);
}
DI __amdgpu_buffer_rsrc_t rsrc(const void* p, unsigned bytes) {
  return __builtin_amdgcn_make_buffer_rsrc(const_cast<void*>(p), 0, bytes, 0x00020000);
}
template <int AUX> DI v4u ld128(__amdgpu_buffer_rsrc_t r, unsigned voff, unsigned soff) {
  return __builtin_amdgcn_raw_buffer_load_b128(r, voff, soff, AUX);
}
template <int AUX> DI v2u ld64(__amdgpu_buffer_rsrc_t r, unsigned voff, unsigned soff) {
  return __builtin_amdgcn_raw_buffer_load_b64(r, voff, soff, AUX);
}
DI void st128(__amdgpu_buffer_rsrc_t r, unsigned voff, unsigned soff, v4u v) { __builtin_amdgcn_raw_buffer_store_b128(v, r, voff, soff, SC1); }
DI void st64(__amdgpu_buffer_rsrc_t r, unsigned voff, unsigned soff, v2u v) { __builtin_amdgcn_raw_buffer_store_b64(v, r, voff, soff, SC1); }
DI v2u trread(LAS unsigned char* p) {
  return __builtin_bit_cast(v2u, __builtin_amdgcn_ds_read_tr16_b64_v4i16((LAS s16x4*)p));
}
DI bool tags_ok(v4u g, unsigned T) { return (g[1] == T) & (g[3] == T); }
DI void wg_barrier() {
  asm volatile("s_waitcnt lgkmcnt(0)" ::: "memory");
  __builtin_amdgcn_s_barrier();
  asm volatile("" ::: "memory");
}
template <int N> DI void wait_vm() { asm volatile("s_waitcnt vmcnt(%0)" ::"i"(N) : "memory"); }
DI unsigned clock_now() { return (unsigned)__builtin_amdgcn_s_memrealtime(); }
DI void hold(unsigned t) {
  asm volatile(
      "1:\n\ts_memrealtime vcc\n\ts_waitcnt lgkmcnt(0)\n\ts_sub_u32 vcc_lo, vcc_lo, %0\n\ts_cmp_lt_i32 vcc_lo, 0\n\t"
      "s_cbranch_scc0 2f\n\ts_sleep 1\n\ts_branch 1b\n2:"
      : : "s"(t) : "vcc", "scc", "memory");
}

DI v4u wfrag(LAS unsigned char* tile, int lane, int ks, int cq) {
  const int g = lane >> 4, q4 = (lane & 15) >> 2, p = lane & 3;
  LAS unsigned char* ta = tile + (32 * ks + 8 * g + q4) * TP_STR + 32 * cq + 8 * p;
  v2u a0 = trread(ta), a1 = trread(ta + 4 * TP_STR);
  return v4u{a0[0], a0[1], a1[0], a1[1]};
}
DI void slice_product(LAS unsigned char* stg, int lane, v4u b0, v4u b1) {
  f32x4 c[4];
#pragma unroll
  for (int cq = 0; cq < 4; ++cq) {
    c[cq] = mfma32(wfrag(stg, lane, 0, cq), b0, f32x4{0, 0, 0, 0});
    c[cq] = mfma32(wfrag(stg, lane, 1, cq), b1, c[cq]);
  }
#pragma unroll
  for (int cq = 0; cq < 4; ++cq) *(LAS f32x4*)(stg + cq * 1024 + lane * 16) = c[cq];
  asm volatile("s_waitcnt lgkmcnt(0)" ::: "memory");
}
template <typename V> DI V slices_sum(LAS unsigned char* lds, int off) {
  V c[8];
#pragma unroll
  for (int u = 0; u < 8; ++u) c[u] = *(LAS V*)(lds + L_STG + u * STG + off);
  return ((c[0] + c[1]) + (c[2] + c[3])) + ((c[4] + c[5]) + (c[6] + c[7]));
}
DI unsigned arrive(LAS unsigned char* cnt, int lane) {
  unsigned arr = 0;
  if (lane == 0) arr = __hip_atomic_fetch_add((LAS unsigned*)cnt, 1u, __ATOMIC_RELAXED, __HIP_MEMORY_SCOPE_WORKGROUP);
  arr = __builtin_amdgcn_readfirstlane(arr);
  asm volatile("" ::: "memory");
  return arr;
}
DI void wait_cnt(LAS unsigned char* cnt, unsigned n) {
  SPIN(CAP_L) {
    const unsigned c = __hip_atomic_load((LAS unsigned*)cnt, __ATOMIC_RELAXED, __HIP_MEMORY_SCOPE_WORKGROUP);
    asm volatile("" ::: "memory");
    if (__builtin_amdgcn_readfirstlane(c) == n) break;
    __builtin_amdgcn_s_sleep(1);
  }
}
template <int I> struct IC { static constexpr int v = I; };
template <int A, int B, typename F> DI void static_for(F&& f) {
  if constexpr (A < B) {
    f(IC<A>{});
    static_for<A + 1, B>(f);
  }
}
constexpr int pv_done(int i, int NS, int NR, int NG) {
  int done = 0;
  for (int k = NR + NG + 1; k <= i; ++k)
    for (int u = 0; u < 3; ++u)
      if (done <= k - NS - WIN) ++done;
  return done;
}

struct Args {
  const uint16_t *kc, *vc, *wq, *w1, *w2, *x, *b1;
  unsigned char* ws;
  const uint16_t *wo, *b2, *gw;
  uint16_t* out;
};
static_assert(sizeof(Args) == 96, "no padding");
struct Role {
  int grp, sp, tile;
  bool isQ, isH, isF;
};
struct Early {
  v4u pf, f4[4], lt[4], bf[2];
  v2u lb1[2];
  unsigned tch, E;
};

template <bool HEAVY>
DI void body(const Args& A, const Role& R, LAS unsigned char* lds, const Early& Y, unsigned long long t0) {
  constexpr int NS = HEAVY ? NSH : NSL, NR = HEAVY ? NRH : NRL, NG = (NS + 3) / 4, QPOS = NR - WIN - 2;
  constexpr int T4 = HEAVY ? TH0 : TL0, G1 = HEAVY ? G1H : G1L;
  const int j = blockIdx.x, tid = threadIdx.x, lane = tid & 63;
  const int w = __builtin_amdgcn_readfirstlane(tid >> 6);
  const int x8 = j & 7, r32 = j >> 3;
  const int sp = R.sp, b = R.grp >> 3, kvh = R.grp & 7;
  const bool isQ = HEAVY && R.isQ, isH = HEAVY && R.isH, isF = HEAVY && R.isF;
  const int fks = (R.tile - 28) >> 3, fns = (R.tile - 28) & 7;
  const int hd = x8 * 4 + (r32 >> 3), ns = (r32 + 6) & 7;
  const int fb = r32 >> 2, fq = r32 & 3;
  const __amdgpu_buffer_rsrc_t rws = rsrc(A.ws, WS_END);
  LAS unsigned char* cnt = lds + L_CNT;

  const __amdgpu_buffer_rsrc_t rk = rsrc(A.kc + (size_t)R.grp * 4096 * 128, 1 << 20), rv = rsrc(A.vc + (size_t)R.grp * 4096 * 128, 1 << 20);
  const unsigned qsel = ((lane & 15) >> 2) * 256 + (lane >> 4) * 64 + (lane & 3) * 16;
  const unsigned sbase = (((sp >> 1) * 64 + (HEAVY ? 0 : NSH)) * 8 + w) * 1024;
  const unsigned koff = qsel + sbase, voff = lane * 16 + sbase;
  v4u kv[NR];
  unsigned tb = (unsigned)t0, tf = 0;
  auto sched = [](int i) constexpr { return T4 + (TEND - T4) * (i - 4) / (2 * NS - 5); };
  auto slot_load = [&](int i) INL {
    if (i >= 4 && i % 2 == 0) {
      if ((i > G1 + WIN && i <= G1 + WIN + 6) || (i >= NR && i < NR + 2)) tb = __builtin_amdgcn_readfirstlane(*(LAS unsigned*)(cnt + C_ORG));
      hold(tb + sched(i));
    }
    kv[i % NR] = i < NS ? ld128<NT>(rk, koff, i * 8192) : ld128<NT>(rv, voff, (i - NS) * 8192);
  };
  LAS unsigned char* stg = lds + L_STG + w * STG;
  unsigned E = Y.E, T;
  auto tag = [&]() INL {
    asm volatile("s_waitcnt lgkmcnt(0)" : "+s"(E) : : "memory");
    T = E * 1664525u + 1013904223u;
  };
  if (tid < 10) *(LAS unsigned*)(cnt + tid * 4) = tid == C_ORG / 4 ? tb : 0;
  wg_barrier();
  if (!HEAVY) {
#pragma unroll
    for (int i = 0; i < 4; ++i) kv[i] = Y.f4[i];
    wait_vm<4>();
    tf = clock_now();
  } else {
#pragma unroll
    for (int i = 0; i < 4; ++i) {
      wait_vm<8>();
      slot_load(i);
      SCHED_FENCE();
    }
    tag();
#pragma unroll
    for (int i = 0; i < 8; ++i) *(LAS v4u*)(stg + (8 * i + (lane >> 3)) * TP_STR + (lane & 7) * 16) = i < 4 ? Y.f4[i] : Y.lt[i - 4];
    if (isQ | isH) {
      slice_product(stg, lane, Y.bf[0], Y.bf[1]);
      if (arrive(cnt + C_TILE, lane) == 0) {
        wait_cnt(cnt + C_TILE, 8);
#pragma unroll
        for (int u = 0; u < 2; ++u) {
          const int cq = 2 * u + ((lane >> 3) & 1);
          f32x4 c = slices_sum<f32x4>(lds, cq * 1024 + (lane & 0x37) * 16);
          if (isH) {
            c[0] = rbf(rbf(c[0]) + bf_lo(Y.lb1[u][0])); c[1] = rbf(rbf(c[1]) + bf_hi(Y.lb1[u][0]));
            c[2] = rbf(rbf(c[2]) + bf_lo(Y.lb1[u][1])); c[3] = rbf(rbf(c[3]) + bf_hi(Y.lb1[u][1]));
#pragma unroll
            for (int r = 0; r < 4; ++r) { c[r] = c[r] > 0.f ? c[r] : 0.f; c[r] = c[r] * c[r]; }
          }
          st128(rws, isQ ? (lane & 7) * 16384 + cq * 64 + (lane >> 4) * 16 : cq * 512 + (lane >> 4) * 128 + (lane & 7) * 16,
                isQ ? WS_QG + R.tile * 256 : WS_HG + R.tile * 2048, v4u{pk_bf16(c[0], c[1]), T, pk_bf16(c[2], c[3]), T});
        }
      }
    }
    asm volatile("" ::"v"(Y.pf), "v"(Y.tch));
  }
  SCHED_FENCE();

  float gate = 0.f;
  v4u qg[2], hg[2], lgx, lgw, tg;
  unsigned lb2;
  bool lead;
  auto rep_load = [&]() INL { tg = ld128<SC1>(rws, lead ? lane * 16 : OOB, WS_T0); };
  auto rep_mean = [&](int late) INL {
    float e[2];
#pragma unroll
    for (int u = 0; u < 2; ++u) e[u] = (float)(tg[2 * u + 1] == T ? min(max((int)(tg[2 * u] - (unsigned)t0), -1000), 1000) : late);
    float d = wave_sum(e[0] + e[1]) * (1.f / 128.f);
    d += wave_sum(fabsf(e[0] - d) + fabsf(e[1] - d)) * (0.5f / 128.f);
    *(LAS unsigned*)(cnt + C_ORG) = (unsigned)t0 + (int)d + OB;
  };
  const bool hpad = lane >= 48;
  auto qh_load = [&]() INL {
#pragma unroll
    for (int u = 0; u < 2; ++u) qg[u] = ld128<SC1>(rws, lead ? lane * 16 + u * 1024 : OOB, WS_QG + (b * 2048 + kvh * 256) * 8);
    if (HEAVY) {
      const unsigned so = isF ? WS_HG + (8 * fks + w) * 1792 : 0;
      hg[0] = ld128<SC1>(rws, isF ? lane * 16 : OOB, so);
      hg[1] = ld128<SC1>(rws, (isF && !hpad) ? lane * 16 + 1024 : OOB, so);
    }
  };
  auto qh_ok = [&]() INL {
    bool ok = !lead | (tags_ok(qg[0], T) & tags_ok(qg[1], T));
    if (HEAVY) ok &= !isF | (tags_ok(hg[0], T) & (hpad | tags_ok(hg[1], T)));
    return __all(ok);
  };
  static_assert(NS <= NR && G1 + WIN <= GPOS && (HEAVY || 5 + WIN <= G1) && GPOS + WIN <= QPOS, "");
  static_for<4, NR>([&](auto ic) INL {
    constexpr int i = decltype(ic)::v;
    if (i == 5 && !HEAVY) {
      tag();
      const bool me = arrive(cnt + C_REP, lane) == 0;
      st64(rws, (me && lane == 0) ? 0 : OOB, WS_T0 + (j >> 1) * 8, v2u{(int)(tf - AL0 - tb) > 0 ? tf - AL0 : tb, T});
      SCHED_FENCE();
    }
    if (i == WIN + 2 && !HEAVY) asm volatile("" ::"v"(Y.pf), "v"(Y.tch));
    if (i == G1) {
      lead = arrive(cnt + C_RLEAD, lane) == 0;
      rep_load();
      SCHED_FENCE();
    }
    if (i == G1 + WIN) {
      if (lead) rep_mean(sched(G1) - 250);
      SCHED_FENCE();
    }
    if (i == GPOS) {
      lgx = ld128<0>(rsrc(A.x, 8192), w == 4 ? lane * 16 : OOB, fb * 1024);
      lgw = ld128<0>(rsrc(A.gw, 1024), w == 4 ? lane * 16 : OOB, 0);
      lb2 = __builtin_amdgcn_raw_buffer_load_b32(rsrc(A.b2, 1024), w == 4 ? (lane & 7) * 4 : OOB, (64 * x8 + 16 * fq) * 2, 0);
      SCHED_FENCE();
    }
    if (i == GPOS + WIN) {
      if (w == 4) {
        float t = 0.f;
#pragma unroll
        for (int u = 0; u < 4; ++u) t += rbf(bf_lo(lgx[u]) * bf_lo(lgw[u])) + rbf(bf_hi(lgx[u]) * bf_hi(lgw[u]));
        t = rbf(wave_sum(t));
        gate = rbf(1.f / (1.f + __expf(-t)));
      }
      asm volatile("" : "+v"(gate));
      SCHED_FENCE();
    }
    if (i == QPOS) {
      lead = arrive(cnt + C_QLEAD, lane) == 0;
      qh_load();
      rep_load();
      SCHED_FENCE();
    }
    if (i == QPOS + WIN) {
      if (!qh_ok()) {
        SPIN(CAP_G) {
          __builtin_amdgcn_s_sleep(2);
          qh_load();
          const bool ok = qh_ok();
          asm volatile("" ::: "memory");
          if (ok) break;
        }
      }
      if (lead) {
#pragma unroll
        for (int u = 0; u < 2; ++u) *(LAS v2u*)(lds + L_QS + u * 512 + lane * 8) = v2u{qg[u][0], qg[u][2]};
        rep_mean(sched(QPOS) - 250);
        asm volatile("s_waitcnt lgkmcnt(0)" ::: "memory");
        __hip_atomic_store((LAS unsigned*)(cnt + C_QRDY), 1u, __ATOMIC_RELAXED, __HIP_MEMORY_SCOPE_WORKGROUP);
      }
      if (HEAVY && isF) {
        v4u hf[2];
        const bool hi = lane & 8;
#pragma unroll
        for (int s = 0; s < 2; ++s) {
          const unsigned o0 = dppu<ROR8>(hg[s][0]), o1 = dppu<ROR8>(hg[s][2]);
          hf[s] = hi ? v4u{o0, o1, hg[s][0], hg[s][2]} : v4u{hg[s][0], hg[s][2], o0, o1};
        }
        slice_product(stg, lane, hf[0], hf[1]);
        arrive(cnt + C_FFN, lane);
      }
      SCHED_FENCE();
    }
    if (i >= QPOS && i < QPOS + WIN) wait_vm<WIN - 1 + (HEAVY ? 5 : 3)>();
    else if (i >= GPOS && i < GPOS + WIN) wait_vm<WIN - 1 + 3>();
    else if ((!HEAVY && i >= 5 && i < 5 + WIN) || (i >= G1 && i < G1 + WIN)) wait_vm<WIN>();
    else wait_vm<WIN - 1>();
    slot_load(i);
    SCHED_FENCE();
  });
  wait_cnt(cnt + C_QRDY, 1);
  const v4u qf = *(LAS v4u*)(lds + L_QS + qsel);
  SCHED_FENCE();

  const int cc = lane & 3;
  const unsigned mask0 = cc == 0 ? 0x0000ffffu : cc == 1 ? 0xffff0000u : 0u;
  const unsigned mask1 = cc == 2 ? 0x0000ffffu : cc == 3 ? 0xffff0000u : 0u;
  constexpr float C2 = 0.08838834764831845f * 1.4426950408889634f;
  float S[NG];
  unsigned pp[NG];
  f32x4 a0 = {0, 0, 0, 0}, a1 = {0, 0, 0, 0};
  auto score = [&](v4u k) INL {
    const f32x4 d = mfma32(k, qf, f32x4{0, 0, 0, 0});
    return (dpp<0x00>(d[0]) + dpp<0x55>(d[1])) + (dpp<0xAA>(d[2]) + dpp<0xFF>(d[3]));
  };
  auto pv = [&](int i) INL {
    const unsigned pb = (i & 3) == 0 ? dppu<0x00>(pp[i >> 2]) : (i & 3) == 1 ? dppu<0x55>(pp[i >> 2]) : (i & 3) == 2 ? dppu<0xAA>(pp[i >> 2]) : dppu<0xFF>(pp[i >> 2]);
    const v2u pa = {pb & mask0, pb & mask1};
    const v4u v = kv[(NS + i) % NR];
    a0 = mfma16(pa, v2u{v[0], v[1]}, a0);
    a1 = mfma16(pa, v2u{v[2], v[3]}, a1);
  };
  const unsigned sf = WS_FG + (x8 * 32 + r32) * 512;
  v4u fg, lo[2];
  float f0 = 0.f, f1 = 0.f;
  bool fok = false;
  auto ffn_part = [&]() INL {
    f0 = lane < 32 ? u2f(fg[0]) : 0.f;
    f1 = lane < 32 ? u2f(fg[2]) : 0.f;
    f0 += dpp<ROR8>(f0); f1 += dpp<ROR8>(f1);
    f0 = xrow_sum(f0); f1 = xrow_sum(f1);
    const float og = rbf(1.f - gate);
    f0 = rbf(og * rbf(rbf(f0) + bf_lo(lb2)));
    f1 = rbf(og * rbf(rbf(f1) + bf_hi(lb2)));
    asm volatile("" : "+v"(f0), "+v"(f1));
  };
  constexpr int FS = NR + 3, FP = 2 * NS - 10 - WIN;
  static_assert(2 * NS - NR >= NG + 2 + WIN && HP <= WIN && FS + WIN + 4 <= FP, "");
  static_for<NR, 2 * NS>([&](auto ic) INL {
    constexpr int i = decltype(ic)::v, t = i - NR;
    if constexpr (t < NG) {
      const float s0 = score(kv[4 * t]), s1 = 4 * t + 1 < NS ? score(kv[(4 * t + 1) % NR]) : -INFINITY;
      const float s2 = 4 * t + 2 < NS ? score(kv[(4 * t + 2) % NR]) : -INFINITY, s3 = 4 * t + 3 < NS ? score(kv[(4 * t + 3) % NR]) : -INFINITY;
      S[t] = (cc & 2) ? ((cc & 1) ? s3 : s2) : ((cc & 1) ? s1 : s0);
    }
    if constexpr (t == NG) {
      float m = S[0];
#pragma unroll
      for (int u = 1; u < NG; ++u) m = fmaxf(m, S[u]);
      m = fmaxf(m, dpp<XOR1>(m));
      m = fmaxf(m, dpp<XOR2>(m));
      m = xrow_max(m) * C2;
      float l = 0.f;
#pragma unroll
      for (int u = 0; u < NG; ++u) {
        const float p = __builtin_amdgcn_exp2f(__builtin_fmaf(S[u], C2, -m));
        l += p;
        pp[u] = pk_bf16(p, p);
      }
      l += dpp<XOR1>(l);
      l += dpp<XOR2>(l);
      l = xrow_sum(l);
      if ((lane & 0x33) == 0) {
        *(LAS float*)(lds + L_ML + (lane >> 2) * 32 + w * 4) = m;
        *(LAS float*)(lds + L_ML + 128 + (lane >> 2) * 32 + w * 4) = l;
      }
      asm volatile("s_waitcnt lgkmcnt(0)" ::: "memory");
      if (arrive(cnt + C_SOFT, lane) == 7) {
        const float mw = *(LAS float*)(lds + L_ML + (lane & 31) * 4), lw = *(LAS float*)(lds + L_ML + 128 + (lane & 31) * 4);
        float M = fmaxf(mw, dpp<XOR1>(mw));
        M = fmaxf(M, dpp<XOR2>(M));
        M = fmaxf(M, dpp<HMIR>(M));
        const float ex = __builtin_amdgcn_exp2f(mw - M);
        float L = ex * lw;
        L += dpp<XOR1>(L);
        L += dpp<XOR2>(L);
        L += dpp<HMIR>(L);
        *(LAS float*)(lds + L_ML + 256 + (lane & 31) * 4) = ex * __builtin_amdgcn_rcpf(L);
        *(LAS float*)(lds + L_ML + 384 + ((lane >> 3) & 3) * 4) = M + __builtin_amdgcn_logf(L);
        asm volatile("s_waitcnt lgkmcnt(0)" ::: "memory");
      }
    }
    if constexpr (HEAVY && i == FS) {
      f32x2 c = {0, 0};
      if (isF && w < 4) {
        wait_cnt(cnt + C_FFN, 8);
        c = slices_sum<f32x2>(lds, w * 1024 + (lane & 0x37) * 16 + (lane & 8));
      }
      st128(rws, (isF && w < 4) ? (lane & 7) * 2048 + (lane >> 4) * 32 + (lane & 8) * 2 : OOB,
            isF ? WS_FG + ((fns * 32 + (w & 3)) * 64 + fks * 16) * 8 : 0, v4u{f2u(c[0]), T, f2u(c[1]), T});
    }
    if constexpr (i == FP) fg = ld128<SC1>(rws, w == 4 ? (lane & 31) * 16 : OOB, sf);
    if constexpr (i == FP + WIN) {
      if (w == 4) {
        fok = __all(tags_ok(fg, T));
        ffn_part();
      }
    }
    SCHED_FENCE();
    if constexpr ((i >= FP && i < FP + WIN) || (HEAVY && i >= FS && i < FS + WIN)) wait_vm<WIN>();
    else wait_vm<WIN - 1>();
    slot_load(i);
    SCHED_FENCE();
    static_for<pv_done(i - 1, NS, NR, NG), pv_done(i, NS, NR, NG)>([&](auto uc) INL { pv(decltype(uc)::v); });
    SCHED_FENCE();
  });
  static_for<pv_done(2 * NS - 1, NS, NR, NG), NS - 1>([&](auto uc) INL {
    constexpr int u = decltype(uc)::v;
    if constexpr (u == NS - HP) {
      const bool last = arrive(cnt + C_ANN1, lane) == 7;
      const unsigned lse = *(LAS unsigned*)(lds + L_ML + 384 + (lane & 3) * 4);
      st64(rws, (last && lane < 4) ? lane * 8 : OOB, WS_LG + ((kvh * 8 + b) * 4 + sp) * 128, v2u{lse, T});
      SCHED_FENCE();
    }
    pv(u);
    SCHED_FENCE();
  });
  asm volatile("" : "+v"(a0), "+v"(a1));
  wait_vm<1>();
#pragma unroll
  for (int u = 0; u < 2; ++u)
    lo[u] = ld128<NT>(rsrc(A.wo, 4096 * 1024), (lane >> 3) * 1024 + (lane & 7) * 16, (128 * hd + 16 * w + 8 * u) * 1024 + ns * 128);
  SCHED_FENCE();
  pv(NS - 1);
  *(LAS f32x4*)(lds + L_MRG + w * 2048 + lane * 32) = a0;
  *(LAS f32x4*)(lds + L_MRG + w * 2048 + lane * 32 + 16) = a1;
  wg_barrier();

  if (w < 4) {
    const f32x4 g0 = *(LAS f32x4*)(lds + L_ML + 256 + w * 32), g1 = *(LAS f32x4*)(lds + L_ML + 256 + w * 32 + 16);
    f32x2 ov[8];
#pragma unroll
    for (int u = 0; u < 8; ++u) ov[u] = *(LAS f32x2*)(lds + L_MRG + u * 2048 + w * 512 + lane * 8);
    const f32x2 o = ((ov[0] * g0[0] + ov[1] * g0[1]) + (ov[2] * g0[2] + ov[3] * g0[3])) + ((ov[4] * g1[0] + ov[5] * g1[1]) + (ov[6] * g1[2] + ov[7] * g1[3]));
    const unsigned pk = pk_bf16(o[0], o[1]), nb = dppu<XOR1>(pk);
    st128(rws, (lane & 1) ? OOB : lane * 8, WS_OG + (((4 * kvh + w) * 8 + b) * 4 + sp) * 512, v4u{pk, T, nb, T});
  }
  SCHED_FENCE();
  asm volatile("" ::: "memory");
  __builtin_amdgcn_s_barrier();
  asm volatile("" ::: "memory");

  v4u wf[4];
  {
    const unsigned so = WS_OG + (hd * 8 + w) * 2048, sl = WS_LG + ((hd >> 2) * 8 + w) * 512 + (hd & 3) * 8;
    v4u og[2];
    v2u lg;
    SPIN(CAP_G) {
      lg = ld64<SC1>(rws, cc * 128, sl);
      const bool ok = __all(lg[1] == T);
      asm volatile("" ::: "memory");
      if (ok) break;
    }
#pragma unroll
    for (int s = 0; s < 2; ++s) og[s] = ld128<SC1>(rws, lane * 16 + s * 1024, so);
    SCHED_FENCE();
    {
      const unsigned now = clock_now();
      st64(rws, (arrive(cnt + C_ANN2, lane) == 7 && lane == 0) ? 0 : OOB, WS_H2 + (ns * 32 + hd) * 128, v2u{now, T});
    }
    const float ls = u2f(lg[0]);
    float M = fmaxf(ls, dpp<XOR1>(ls));
    M = fmaxf(M, dpp<XOR2>(M));
    const float ex = __builtin_amdgcn_exp2f(ls - M);
    float Z = ex + dpp<XOR1>(ex);
    Z += dpp<XOR2>(Z);
    const float wn = ex * __builtin_amdgcn_rcpf(Z);
    const float m0 = dpp<0x00>(wn), m1 = dpp<0x55>(wn), m2 = dpp<0xAA>(wn), m3 = dpp<0xFF>(wn);
    float w0 = lane < 32 ? m0 : m1, w1 = lane < 32 ? m2 : m3;
    asm volatile("" : "+v"(w0), "+v"(w1));
#pragma unroll
    for (int u = 0; u < 2; ++u) *(LAS v4u*)(lds + L_TO + (16 * w + 8 * u + (lane >> 3)) * TP_STR + (lane & 7) * 16) = lo[u];
    wg_barrier();
#pragma unroll
    for (int s = 0; s < 4; ++s) wf[s] = wfrag(lds + L_TO, lane, s, w & 3);
    SCHED_FENCE();
    if (!__all(tags_ok(og[0], T) & tags_ok(og[1], T))) {
      SPIN(CAP_G) {
#pragma unroll
        for (int s = 0; s < 2; ++s) og[s] = ld128<SC1>(rws, lane * 16 + s * 1024, so);
        const bool ok = __all(tags_ok(og[0], T) & tags_ok(og[1], T));
        asm volatile("" ::: "memory");
        if (ok) break;
      }
    }
    const float o0 = w0 * bf_lo(og[0][0]) + w1 * bf_lo(og[1][0]), o1 = w0 * bf_hi(og[0][0]) + w1 * bf_hi(og[1][0]);
    const float o2 = w0 * bf_lo(og[0][2]) + w1 * bf_lo(og[1][2]), o3 = w0 * bf_hi(og[0][2]) + w1 * bf_hi(og[1][2]);
    auto x0 = __builtin_amdgcn_permlane32_swap(f2u(o0), f2u(o2), false, false);
    auto x1 = __builtin_amdgcn_permlane32_swap(f2u(o1), f2u(o3), false, false);
    *(LAS unsigned*)(lds + L_OS + w * OS_STR + (lane & 31) * 8 + (lane >> 5) * 4) = pk_bf16(u2f(x0[0]) + u2f(x0[1]), u2f(x1[0]) + u2f(x1[1]));
  }
  wg_barrier();

  if (w < 4) {
    v4u ob[4];
#pragma unroll
    for (int s = 0; s < 4; ++s) ob[s] = *(LAS v4u*)(lds + L_OS + (lane & 7) * OS_STR + s * 64 + (lane >> 4) * 16);
    f32x4 c = mfma32(wf[0], ob[0], f32x4{0, 0, 0, 0}), c2 = mfma32(wf[1], ob[1], f32x4{0, 0, 0, 0});
    c = mfma32(wf[2], ob[2], c);
    c2 = mfma32(wf[3], ob[3], c2);
    c += c2;
    const bool hi = lane & 8;
    st128(rws, (lane & 7) * 16384 + (lane >> 4) * 32 + (lane & 8) * 2, WS_PG + ((ns * 32 + w) * 512 + hd * 16) * 8,
          v4u{f2u(hi ? c[2] : c[0]), T, f2u(hi ? c[3] : c[1]), T});
  } else if (w == 4) {
    const unsigned sp_ = WS_PG + (x8 * 32 + r32) * 4096;
    if (!fok) {
      SPIN(CAP_G) {
        fg = ld128<SC1>(rws, (lane & 31) * 16, sf);
        const bool ok = __all(tags_ok(fg, T));
        asm volatile("" ::: "memory");
        if (ok) break;
      }
      ffn_part();
    }
    v2u hn;
    SPIN(CAP_G) {
      hn = ld64<SC1>(rws, (lane & 31) * 128, WS_H2 + x8 * 4096);
      asm volatile("" ::: "memory");
      if (__all(hn[1] == T)) break;
    }
    {
      int d = (int)(hn[0] - tb);
      d = max(d, (int)dppu<XOR1>(d));
      d = max(d, (int)dppu<XOR2>(d));
      d = max(d, (int)dppu<HMIR>(d));
      d = max(d, (int)dppu<MIR>(d));
      hold(tb + min(max(__builtin_amdgcn_readlane(d, 0), __builtin_amdgcn_readlane(d, 16)), 10000) + C2L);
    }
    v4u pg[4];
    SPIN(CAP_G) {
#pragma unroll
      for (int i = 0; i < 4; ++i) pg[i] = ld128<SC1>(rws, lane * 16 + i * 1024, sp_);
      const bool ok = __all(tags_ok(pg[0], T) & tags_ok(pg[1], T) & tags_ok(pg[2], T) & tags_ok(pg[3], T));
      asm volatile("" ::: "memory");
      if (ok) break;
    }
    float s0 = (u2f(pg[0][0]) + u2f(pg[1][0])) + (u2f(pg[2][0]) + u2f(pg[3][0]));
    float s1 = (u2f(pg[0][2]) + u2f(pg[1][2])) + (u2f(pg[2][2]) + u2f(pg[3][2]));
    s0 += dpp<ROR8>(s0); s1 += dpp<ROR8>(s1);
    s0 = xrow_sum(s0); s1 = xrow_sum(s1);
    const float r0 = rbf(gate * rbf(s0)) + f0;
    const float r1 = rbf(gate * rbf(s1)) + f1;
    __builtin_amdgcn_raw_buffer_store_b32(pk_bf16(r0, r1), rsrc(A.out, 8192), lane < 8 ? lane * 4 : OOB, (fb * 512 + 64 * x8 + 16 * fq) * 2, SC1);
    __builtin_amdgcn_raw_buffer_store_b32(T, rws, (j == 0 && lane == 0) ? 0 : OOB, 0, SC1);
  }
}

__global__ void __launch_bounds__(512, 2) fused_decode(Args) {
  __shared__ __attribute__((aligned(16))) unsigned char lds_raw[L_END];
  unsigned long long t0;
  u64x8 ka;
  u64x4 kb;
  asm volatile("s_load_dwordx16 %0, %3, 0x0\n\ts_load_dwordx8 %1, %3, 0x40\n\ts_memrealtime %2"
               : "=&s"(ka), "=&s"(kb), "=&s"(t0) : "s"(__builtin_amdgcn_kernarg_segment_ptr()));
  SCHED_FENCE();
  unsigned long long pc;
  asm volatile("s_getpc_b64 %0" : "=s"(pc));
  Early Y;
  Y.pf = ld128<0>(rsrc((const void*)(pc & ~255ull), CODEB), blockIdx.x < 32 ? threadIdx.x * 16 + (blockIdx.x >> 3) * 8192 : OOB, 0);
  SCHED_FENCE();
  const int j = blockIdx.x, lane = threadIdx.x & 63, w = __builtin_amdgcn_readfirstlane(threadIdx.x >> 6);
  const int x8 = j & 7, r32 = j >> 3;
  Role R;
  R.grp = x8 * 8 + (r32 >> 2);
  R.sp = (r32 + x8) & 3;
  R.tile = (r32 >> 2) * 8 + ((x8 + (r32 >> 2)) & 7);
  const bool heavy = (R.sp & 1) == 0;
  R.isQ = R.sp == 0;
  R.isH = R.sp == 2 && R.tile < 28;
  R.isF = R.sp == 2 && R.tile >= 28 && R.tile < 60;
  unsigned oK = (R.grp << 20) + (((R.sp >> 1) * 64 + NSH) * 8 + w) * 1024, oQ = w * (64 * 8192) + R.tile * 128;
  unsigned oH = w * (64 * 3584) + R.tile * 128, oF = (448 * ((R.tile - 28) >> 3) + 56 * w) * 1024 + ((R.tile - 28) & 7) * 128;
  asm volatile("" : "+s"(oK), "+s"(oQ), "+s"(oH), "+s"(oF));
  const bool off = heavy && !(R.isQ | R.isH | R.isF);
  unsigned rs = off ? 0 : R.isQ ? 8192 : R.isH ? 3584 : 1024;
  unsigned bytes = off ? 0 : !heavy ? 1 << 26 : R.isQ ? 512 * 8192 : 1792 * 1024;
  unsigned org = !heavy ? oK : R.isQ ? oQ : R.isH ? oH : oF;
  asm volatile("" : "+s"(rs), "+s"(bytes), "+s"(org));
  unsigned lk = ((lane & 15) >> 2) * 256 + (lane >> 4) * 64 + (lane & 3) * 16, lw = (lane >> 3) * rs + (lane & 7) * 16;
  asm volatile("" : "+v"(lk), "+v"(lw));
  unsigned vo = org + (heavy ? lw : lk);
  asm volatile("" : "+v"(vo));
  SCHED_FENCE();
  asm volatile("s_waitcnt lgkmcnt(0)" : "+s"(ka), "+s"(kb), "+s"(t0));
  Args A;
  A.kc = (const uint16_t*)ka[0]; A.vc = (const uint16_t*)ka[1]; A.wq = (const uint16_t*)ka[2]; A.w1 = (const uint16_t*)ka[3];
  A.w2 = (const uint16_t*)ka[4]; A.x = (const uint16_t*)ka[5]; A.b1 = (const uint16_t*)ka[6]; A.ws = (unsigned char*)ka[7];
  A.wo = (const uint16_t*)kb[0]; A.b2 = (const uint16_t*)kb[1]; A.gw = (const uint16_t*)kb[2]; A.out = (uint16_t*)kb[3];
  asm volatile("s_load_dword %0, %1, 0x0" : "=s"(Y.E) : "s"(A.ws) : "memory");
  const __amdgpu_buffer_rsrc_t r0 = rsrc(!heavy ? A.kc : R.isQ ? A.wq : R.isH ? A.w1 : A.w2, bytes);
#pragma unroll
  for (int i = 0; i < 4; ++i) Y.f4[i] = ld128<NT>(r0, vo, 8 * i * rs);
  SCHED_FENCE();
  if (heavy) {
#pragma unroll
    for (int i = 0; i < 4; ++i) Y.lt[i] = ld128<NT>(r0, (i == 3 && R.isF) ? OOB : vo, 8 * (i + 4) * rs);
#pragma unroll
    for (int s = 0; s < 2; ++s) Y.bf[s] = ld128<0>(rsrc(A.x, 8192), (lane & 7) * 1024 + (lane >> 4) * 16 + s * 64, w * 128);
#pragma unroll
    for (int u = 0; u < 2; ++u) Y.lb1[u] = ld64<0>(rsrc(A.b1, 1792 * 2), R.isH ? ((lane >> 3) & 1) * 32 + (lane >> 4) * 8 + u * 64 : OOB, R.isH ? R.tile * 128 : 0);
  }
  SCHED_FENCE();
  {
    unsigned long long c0 = ka[1] + oK, c1 = ka[0] + oK, c2 = kb[0] + (x8 * 4 + (r32 >> 3)) * 131072, c3 = ka[7] + 1024;
    asm volatile("" : "+s"(c0), "+s"(c1), "+s"(c2), "+s"(c3));
    unsigned long long d0 = (w & 1) ? c1 : c0, d1 = (w & 1) ? c3 : c2, d2 = (w & 1) ? kb[3] : ka[5], d3 = (w & 1) ? kb[1] : kb[2];
    asm volatile("" : "+s"(d0), "+s"(d1), "+s"(d2), "+s"(d3));
    d0 = (w & 2) ? d1 : d0;
    d2 = (w & 2) ? d3 : d2;
    asm volatile("" : "+s"(d0), "+s"(d2));
    Y.tch = __builtin_amdgcn_raw_buffer_load_b32(rsrc((const void*)((w & 4) ? d2 : d0), 4), lane * 4, 0, NT);
  }
  SCHED_FENCE();
  if (__builtin_expect(heavy, 0)) body<true>(A, R, (LAS unsigned char*)lds_raw, Y, t0);
  else body<false>(A, R, (LAS unsigned char*)lds_raw, Y, t0);
}

extern "C" void kernel_launch(void* const* d_in, const int* in_sizes, int n_in, void* d_out, int out_size, void* d_ws, size_t ws_size,
                              hipStream_t stream) {
  Args A;
  A.x = (const uint16_t*)d_in[0]; A.kc = (const uint16_t*)d_in[1]; A.vc = (const uint16_t*)d_in[2];
  A.wq = (const uint16_t*)d_in[3]; A.wo = (const uint16_t*)d_in[4]; A.w1 = (const uint16_t*)d_in[6];
  A.b1 = (const uint16_t*)d_in[7]; A.w2 = (const uint16_t*)d_in[8]; A.b2 = (const uint16_t*)d_in[9];
  A.gw = (const uint16_t*)d_in[10]; A.out = (uint16_t*)d_out; A.ws = (unsigned char*)d_ws;
  fused_decode<<<256, 512, 0, stream>>>(A);
}
```
